```python
import torch

def _softmax(x, axis=-1):
    return torch.softmax(x, dim=axis)

def _gelu(x):
    return torch.nn.functional.gelu(x, approximate='tanh')

def _silu(x):
    return torch.nn.functional.silu(x)

def _sigmoid(x):
    return torch.sigmoid(x)

def _layernorm(x, axis=-1, eps=1e-05):
    m = x.mean(dim=axis, keepdim=True)
    v = x.var(dim=axis, keepdim=True, unbiased=False)
    return (x - m) / torch.sqrt(v + eps)

def _scatter_add(tgt, idx, src, axis=0):
    return tgt.clone().index_add_(axis, idx, src)

def _segment_sum(src, seg, n):
    out = torch.zeros((n,) + src.shape[1:], dtype=src.dtype)
    return out.index_add_(0, seg, src)

def _pad(x, pw):
    flat = tuple((v for pair in reversed(pw) for v in pair))
    return torch.nn.functional.pad(x, flat)
kernel_name = 'adaLN_cond__sliding_block_cached__swiglu_ffn__prompt'

def setup_inputs():

    _w = lambda a: (lambda t: t.to(torch.bfloat16) if t.is_floating_point() else t)(torch.as_tensor(a))

    def _A_setup():
        B, Sq, D = (1, 2048, 576)
        Hq, Hkv, Sk, Dh = (32, 16, 2048, 128)
        assert Hq % Hkv == 0, (Hq, Hkv)
        return {'x': _w((lambda *s: torch.randn(*s, dtype=torch.bfloat16) * 0.05)(B, Sq, D)), 'k_cache': _w((lambda *s: torch.randn(*s, dtype=torch.bfloat16) * 3.2)(B, Hkv, Sk, Dh)), 'v_cache': _w((lambda *s: torch.randn(*s, dtype=torch.bfloat16) * 0.4)(B, Hkv, Sk, Dh)), 'wq': _w((lambda *s: torch.randn(*s, dtype=torch.bfloat16) * 3.2)(D, Hq * Dh) * D ** (-0.5)), 'wo': _w((lambda *s: torch.randn(*s, dtype=torch.bfloat16) * 0.4)(Hq * Dh, D) * (Hq * Dh) ** (-0.5))}
    _A_in = _A_setup()

    def _B_setup():
        B, S, D, F = (1, 2048, 576, 4608)
        return {'x': _w((lambda *s: torch.randn(*s, dtype=torch.bfloat16) * 0.05)(B, S, D)), 'w_gate': _w((lambda *s: torch.randn(*s, dtype=torch.bfloat16) * 0.05)(D, F)), 'w_up': _w((lambda *s: torch.randn(*s, dtype=torch.bfloat16) * 0.05)(D, F)), 'w_down': _w((lambda *s: torch.randn(*s, dtype=torch.bfloat16) * 0.05)(F, D))}
    _B_in = _B_setup()
    return {'A_x': _A_in['x'], 'A_k_cache': _A_in['k_cache'], 'A_v_cache': _A_in['v_cache'], 'A_wq': _A_in['wq'], 'A_wo': _A_in['wo'], 'B_x': _B_in['x'], 'B_w_gate': _B_in['w_gate'], 'B_w_up': _B_in['w_up'], 'B_w_down': _B_in['w_down'], 'c': _w((lambda *s: torch.randn(*s, dtype=torch.bfloat16) * 0.1)(1, 576) * 8.0), 'w_mod': _w((lambda *s: torch.randn(*s, dtype=torch.bfloat16) * 0.1)(576, 6 * 576) * 576 ** (-0.5) * 8.0)}

def reference(A_x, A_k_cache, A_v_cache, A_wq, A_wo, B_x, B_w_gate, B_w_up, B_w_down, c, w_mod):

    def _A_ref(x, k_cache, v_cache, wq, wo):
        B, Sq, D = x.shape
        Hq, Dh = (32, 128)
        Hkv, Sk = (k_cache.shape[1], k_cache.shape[2])
        W = min(4096, max(256, Sk // 4))
        rep = Hq // Hkv
        q = (lambda x, *d: x.permute(*d))((x @ wq).reshape(B, Sq, Hq, Dh), 0, 2, 1, 3)
        k = (lambda x, n, axis: torch.repeat_interleave(x, n, dim=axis))(k_cache[:, :, Sk - W:, :], rep, axis=1)
        v = (lambda x, n, axis: torch.repeat_interleave(x, n, dim=axis))(v_cache[:, :, Sk - W:, :], rep, axis=1)
        scores = torch.einsum('bhqd,bhkd->bhqk', q, k) * Dh ** (-0.5)
        p = _softmax(scores, axis=-1)
        o = torch.einsum('bhqk,bhkd->bhqd', p, v)
        return (lambda x, *d: x.permute(*d))(o, 0, 2, 1, 3).reshape(B, Sq, Hq * Dh) @ wo

    def _B_ref(x, w_gate, w_up, w_down):
        g = _silu(x @ w_gate)
        u = x @ w_up
        return g * u @ w_down
    x = A_x
    mod = (_silu(c) @ w_mod).reshape(c.shape[0], 6, 1, -1)
    sa, ca, ga, sb, cb, gb = (mod[:, 0].clone(), mod[:, 1].clone(), mod[:, 2].clone(), mod[:, 3].clone(), mod[:, 4].clone(), mod[:, 5].clone())
    h = _layernorm(x, axis=-1) * (1 + ca) + sa
    x = x + ga * _A_ref(h, A_k_cache, A_v_cache, A_wq, A_wo)
    h = _layernorm(x, axis=-1) * (1 + cb) + sb
    x = x + gb * _B_ref(h, B_w_gate, B_w_up, B_w_down)
    return x
```

```cpp
#include <hip/hip_runtime.h>
#include <stdint.h>
#include <type_traits>

typedef short    bf16x8 __attribute__((ext_vector_type(8)));
typedef short    s16x4  __attribute__((ext_vector_type(4)));
typedef float    f32x16 __attribute__((ext_vector_type(16)));
typedef float    f32x4  __attribute__((ext_vector_type(4)));
typedef float    f32x2  __attribute__((ext_vector_type(2)));
typedef __bf16   bf16x2 __attribute__((ext_vector_type(2)));
typedef unsigned u32x4  __attribute__((ext_vector_type(4)));
typedef unsigned u32x2  __attribute__((ext_vector_type(2)));
typedef unsigned short u16;
#define LAS __attribute__((address_space(3)))
typedef LAS char* lptr;
typedef __amdgpu_buffer_rsrc_t rsrc_t;
#define DI __device__ __forceinline__
#define AI __attribute__((always_inline))
#define SBAR() __builtin_amdgcn_sched_barrier(0)
#define PIN(x) asm volatile("" : "+v"(x))
#define MFMA(a, b, c) __builtin_amdgcn_mfma_f32_32x32x16_bf16((a), (b), (c), 0, 0, 0)

constexpr int S_ = 2048, D_ = 576, HQ = 32, HKV = 16, SK = 2048, DH = 128, WIN = 512, F_ = 4608;
constexpr int NKS = D_ / 16;
constexpr int NRG = S_ / 32;
constexpr float LN_EPS = 1e-5f;
constexpr float QSCALE = 0.08838834764831845f * 1.4426950408889634f;

constexpr size_t WS_MODP = 0;
constexpr size_t WS_XH   = 32768;
constexpr size_t ACT_B   = (size_t)S_ * D_ * 2;
constexpr size_t WS_H2   = WS_XH + ACT_B;
constexpr size_t WS_X2   = WS_H2 + ACT_B;
constexpr size_t WS_P    = WS_X2 + 2 * ACT_B;
constexpr size_t WS_P2   = WS_P + 16 * ACT_B;
constexpr size_t WS_DBG  = WS_P2 + 16 * ACT_B;

DI unsigned rfl(unsigned x) { return __builtin_amdgcn_readfirstlane(x); }
DI unsigned cvtpk(float lo, float hi) { f32x2 v = {lo, hi}; bf16x2 b = __builtin_convertvector(v, bf16x2); return __builtin_bit_cast(unsigned, b); }
DI float bflo(unsigned w) { return __uint_as_float(w << 16); }
DI float bfhi(unsigned w) { return __uint_as_float(w & 0xffff0000u); }
DI float bfr(float x) { return bflo(cvtpk(x, x)); }
DI rsrc_t mkrsrc(const void* p, unsigned bytes) { return __builtin_amdgcn_make_buffer_rsrc((void*)p, (short)0, (int)bytes, 0x00020000); }
template <int OFF> DI void dma16(unsigned m0v, unsigned voff, rsrc_t r, unsigned soff) {
  asm volatile("s_mov_b32 m0, %0\n\ts_nop 0\n\tbuffer_load_dwordx4 %1, %2, %3 offen offset:%4 lds"
               :: "s"(m0v), "v"(voff), "s"(r), "s"(soff), "i"(OFF) : "memory");
}
template <int OFF> DI void gld16(u32x4& d, unsigned voff, rsrc_t r, unsigned soff) {
  asm volatile("buffer_load_dwordx4 %0, %1, %2, %3 offen offset:%4" : "=v"(d) : "v"(voff), "s"(r), "s"(soff), "i"(OFF) : "memory");
}
DI s16x4 trd(lptr p) { return __builtin_amdgcn_ds_read_tr16_b64_v4i16((LAS s16x4*)p); }
DI bf16x8 trfrag(lptr p0, lptr p1) { s16x4 lo = trd(p0), hi = trd(p1); return __builtin_shufflevector(lo, hi, 0, 1, 2, 3, 4, 5, 6, 7); }
DI bf16x8 ldfrag(lptr p) { return *(LAS bf16x8*)p; }
DI bf16x8 asfrag(u32x4 w) { return __builtin_bit_cast(bf16x8, w); }
DI float swap_max(float m) { auto rr = __builtin_amdgcn_permlane32_swap(__float_as_uint(m), __float_as_uint(m), false, false);
  return __builtin_fmaxf(__uint_as_float(rr[0]), __uint_as_float(rr[1])); }
DI float swap_add(float m) { auto rr = __builtin_amdgcn_permlane32_swap(__float_as_uint(m), __float_as_uint(m), false, false);
  return __uint_as_float(rr[0]) + __uint_as_float(rr[1]); }
DI u32x4 pack8(const f32x16& a, int s) {
  u32x4 w = {cvtpk(a[8 * s], a[8 * s + 1]), cvtpk(a[8 * s + 2], a[8 * s + 3]), cvtpk(a[8 * s + 4], a[8 * s + 5]), cvtpk(a[8 * s + 6], a[8 * s + 7])};
  return w;
}
DI lptr lp(unsigned a) { return (lptr)(uintptr_t)a; }
DI void bump(unsigned& a, int d) { a += d; PIN(a); }
template <int N> DI void wait_bar() { asm volatile("s_waitcnt vmcnt(%0) lgkmcnt(0)\n\ts_barrier" :: "i"(N) : "memory"); }
template <int I, int N, class F> DI void sfor(F&& f) { if constexpr (I < N) { f(std::integral_constant<int, I>{}); sfor<I + 1, N>(f); } }

#define STAMP(i)

DI float block_rowsum(float v, float* red, int tid) {
  v += __shfl_xor(v, 8); v += __shfl_xor(v, 16); v += __shfl_xor(v, 32);
  if ((tid & 63) < 8) red[(tid >> 6) * 8 + (tid & 7)] = v;
  __syncthreads();
  float s = 0.f;
#pragma unroll
  for (int i = 0; i < 9; ++i) s += red[i * 8 + (tid & 7)];
  __syncthreads();
  return s;
}

__global__ __launch_bounds__(576) void k0_prep(const u16* __restrict__ x, const u16* __restrict__ c, const u16* __restrict__ wmod,
                                               float* __restrict__ modp, u16* __restrict__ xh) {
  __shared__ float red[9 * 64];
  const int tid = threadIdx.x;
  if (blockIdx.x < 108) {
    const int cg = blockIdx.x >> 1, kq = blockIdx.x & 1, cc = tid & 7, kk = tid >> 3;
    u32x4 w[4]; float sc[4];
#pragma unroll
    for (int i = 0; i < 4; ++i) {
      const int k = kq * 288 + kk + 72 * i;
      w[i] = *(const u32x4*)(wmod + (size_t)k * 3456 + cg * 64 + cc * 8);
      const float cv = bflo(c[k]);
      sc[i] = bfr(cv / (1.f + __expf(-cv)));
    }
    float a[8];
#pragma unroll
    for (int e = 0; e < 8; ++e) a[e] = 0.f;
#pragma unroll
    for (int i = 0; i < 4; ++i)
#pragma unroll
      for (int e = 0; e < 4; ++e) { a[2 * e] += sc[i] * bflo(w[i][e]); a[2 * e + 1] += sc[i] * bfhi(w[i][e]); }
#pragma unroll
    for (int e = 0; e < 8; ++e) { a[e] += __shfl_xor(a[e], 8); a[e] += __shfl_xor(a[e], 16); a[e] += __shfl_xor(a[e], 32); }
    if ((tid & 63) < 8) {
#pragma unroll
      for (int e = 0; e < 8; ++e) red[(tid >> 6) * 64 + cc * 8 + e] = a[e];
    }
    __syncthreads();
    if (tid < 64) {
      float s = 0.f;
#pragma unroll
      for (int i = 0; i < 9; ++i) s += red[i * 64 + tid];
      modp[kq * 3456 + cg * 64 + tid] = s;
    }
  } else {
    const int b = blockIdx.x - 108, row = b * 8 + (tid & 7), ci = tid >> 3, ks = ci >> 1, h = ci & 1;
    const u16* xr = x + (size_t)row * D_ + 16 * ks + 4 * h;
    const u32x2 a0 = *(const u32x2*)xr, a1 = *(const u32x2*)(xr + 8);
    float v[8] = {bflo(a0[0]), bfhi(a0[0]), bflo(a0[1]), bfhi(a0[1]), bflo(a1[0]), bfhi(a1[0]), bflo(a1[1]), bfhi(a1[1])};
    float s = 0.f;
#pragma unroll
    for (int e = 0; e < 8; ++e) s += v[e];
    const float mean = block_rowsum(s, red, tid) * (1.f / D_);
    float q = 0.f;
#pragma unroll
    for (int e = 0; e < 8; ++e) { v[e] -= mean; q += v[e] * v[e]; }
    const float rstd = rsqrtf(block_rowsum(q, red, tid) * (1.f / D_) + LN_EPS);
    u32x4 o = {cvtpk(v[0] * rstd, v[1] * rstd), cvtpk(v[2] * rstd, v[3] * rstd), cvtpk(v[4] * rstd, v[5] * rstd), cvtpk(v[6] * rstd, v[7] * rstd)};
    *(u32x4*)(xh + ((size_t)((row >> 5) * NKS + ks) * 64 + h * 32 + (row & 31)) * 8) = o;
  }
}

template <bool FINAL>
__global__ __launch_bounds__(576) void k_reduce(const u16* __restrict__ part, const u16* __restrict__ x, const float* __restrict__ modp,
                                                float* __restrict__ x2, u16* __restrict__ h2, u16* __restrict__ out) {
  __shared__ float red[9 * 8];
  const int tid = threadIdx.x, row = blockIdx.x * 8 + (tid & 7), ci = tid >> 3, ks = ci >> 1, h = ci & 1;
  const size_t fo = ((size_t)((row >> 5) * NKS + ks) * 64 + h * 32 + (row & 31)) * 8;
  u32x4 p[16];
#pragma unroll
  for (int i = 0; i < 16; ++i) p[i] = *(const u32x4*)(part + (size_t)i * S_ * D_ + fo);
  const int n0 = 16 * ks + 4 * h;
  constexpr int GI = FINAL ? 5 : 2;
  float gate[8], res[8];
  { const f32x4 g0 = *(const f32x4*)(modp + GI * D_ + n0), g1 = *(const f32x4*)(modp + GI * D_ + n0 + 8);
    const f32x4 g2 = *(const f32x4*)(modp + 3456 + GI * D_ + n0), g3 = *(const f32x4*)(modp + 3456 + GI * D_ + n0 + 8);
#pragma unroll
    for (int e = 0; e < 4; ++e) { gate[e] = bfr(g0[e] + g2[e]); gate[4 + e] = bfr(g1[e] + g3[e]); } }
  if constexpr (FINAL) {
    const f32x4 r0 = *(const f32x4*)(x2 + fo), r1 = *(const f32x4*)(x2 + fo + 4);
#pragma unroll
    for (int e = 0; e < 4; ++e) { res[e] = r0[e]; res[4 + e] = r1[e]; }
  } else {
    const u16* xr = x + (size_t)row * D_ + n0;
    const u32x2 a0 = *(const u32x2*)xr, a1 = *(const u32x2*)(xr + 8);
    res[0] = bflo(a0[0]); res[1] = bfhi(a0[0]); res[2] = bflo(a0[1]); res[3] = bfhi(a0[1]);
    res[4] = bflo(a1[0]); res[5] = bfhi(a1[0]); res[6] = bflo(a1[1]); res[7] = bfhi(a1[1]);
  }
  float v[8];
#pragma unroll
  for (int e = 0; e < 8; ++e) v[e] = 0.f;
#pragma unroll
  for (int i = 0; i < 16; ++i)
#pragma unroll
    for (int e = 0; e < 4; ++e) { v[2 * e] += bflo(p[i][e]); v[2 * e + 1] += bfhi(p[i][e]); }
#pragma unroll
  for (int e = 0; e < 8; ++e) v[e] = res[e] + gate[e] * v[e];
  if constexpr (FINAL) {
    u16* o = out + (size_t)row * D_ + n0;
    u32x2 o0 = {cvtpk(v[0], v[1]), cvtpk(v[2], v[3])}, o1 = {cvtpk(v[4], v[5]), cvtpk(v[6], v[7])};
    *(u32x2*)o = o0; *(u32x2*)(o + 8) = o1;
  } else {
    f32x4 s0 = {v[0], v[1], v[2], v[3]}, s1 = {v[4], v[5], v[6], v[7]};
    *(f32x4*)(x2 + fo) = s0; *(f32x4*)(x2 + fo + 4) = s1;
    float cm[8], sh[8];
    { const f32x4 a0 = *(const f32x4*)(modp + 3 * D_ + n0), a1 = *(const f32x4*)(modp + 3 * D_ + n0 + 8);
      const f32x4 a2 = *(const f32x4*)(modp + 3456 + 3 * D_ + n0), a3 = *(const f32x4*)(modp + 3456 + 3 * D_ + n0 + 8);
      const f32x4 b0 = *(const f32x4*)(modp + 4 * D_ + n0), b1 = *(const f32x4*)(modp + 4 * D_ + n0 + 8);
      const f32x4 b2 = *(const f32x4*)(modp + 3456 + 4 * D_ + n0), b3 = *(const f32x4*)(modp + 3456 + 4 * D_ + n0 + 8);
#pragma unroll
      for (int e = 0; e < 4; ++e) { sh[e] = bfr(a0[e] + a2[e]); sh[4 + e] = bfr(a1[e] + a3[e]);
        cm[e] = bfr(1.f + bfr(b0[e] + b2[e])); cm[4 + e] = bfr(1.f + bfr(b1[e] + b3[e])); } }
    float s = 0.f;
#pragma unroll
    for (int e = 0; e < 8; ++e) s += v[e];
    const float mean = block_rowsum(s, red, tid) * (1.f / D_);
    float q = 0.f;
#pragma unroll
    for (int e = 0; e < 8; ++e) { v[e] -= mean; q += v[e] * v[e]; }
    const float rstd = rsqrtf(block_rowsum(q, red, tid) * (1.f / D_) + LN_EPS);
#pragma unroll
    for (int e = 0; e < 8; ++e) v[e] = v[e] * rstd * cm[e] + sh[e];
    u32x4 o = {cvtpk(v[0], v[1]), cvtpk(v[2], v[3]), cvtpk(v[4], v[5]), cvtpk(v[6], v[7])};
    *(u32x4*)(h2 + fo) = o;
  }
}

constexpr int A_SLOT = 32768, A_SPARE = 4 * A_SLOT, A_LDS = 5 * A_SLOT;
constexpr float L_LIMIT = 1.152921504606847e18f;

struct AttnState {
  f32x16 O[4], S[2], negm; u32x4 Pk[2][2]; bf16x8 Q[8], F[8]; float l;
};

__global__ __launch_bounds__(512, 2) void k1_attn(const u16* __restrict__ xh, const u16* __restrict__ wq, const u16* __restrict__ kc,
                                                  const u16* __restrict__ vc, const u16* __restrict__ wo, const float* __restrict__ modp,
                                                  u16* __restrict__ part, unsigned long long* dbg) {
  extern __shared__ __attribute__((aligned(16))) char smem[];
  const lptr L = (lptr)smem;
  const unsigned lds0 = (unsigned)(uintptr_t)L;
  const int tid = threadIdx.x, lane = tid & 63;
  const unsigned w = rfl(tid >> 6), g = w & 3, hd = w >> 2;
  const unsigned j = 2 * (blockIdx.x & 7) + ((blockIdx.x >> 3) & 1), rb = blockIdx.x >> 4, rg = rb * 4 + g;
  const int r32 = lane & 31, h = lane >> 5, r8 = (lane >> 2) & 7, cp = lane & 3, blk = (lane >> 4) & 1, q4 = (lane >> 2) & 3;
  STAMP(0);
  const rsrc_t rWq = mkrsrc(wq, D_ * 4096 * 2), rK = mkrsrc(kc, HKV * SK * DH * 2), rV = mkrsrc(vc, HKV * SK * DH * 2),
               rWo = mkrsrc(wo, 4096 * D_ * 2), rX = mkrsrc(xh, S_ * D_ * 2);
  const unsigned voffWq = (8 * w + r8) * 8192 + h * 64 + cp * 16 + 512 * j;
  const unsigned voffX = lane * 16 + rg * (NKS * 1024);
  const unsigned kvrow = 8 * (w >> 1) + r8;
  const unsigned voffV = kvrow * 256 + (2 * (w & 1) + h) * 64 + cp * 16;
  const unsigned voffK = kvrow * 256 + (2 * (w & 1) + h) * 64 + ((cp ^ ((kvrow >> 2) & 3)) * 16);
  const unsigned kvbase = (j * SK + (SK - WIN)) * 256;
  const unsigned voffWo = (256 * j + 32 * w + r8) * 1152 + h * 64 + cp * 16;
  const unsigned m0w4 = lds0 + w * 4096, m0w1 = lds0 + w * 1024;

  auto issue = [&](auto Cc, auto Pc) AI {
    constexpr int C = decltype(Cc)::value, P = decltype(Pc)::value, slot = (C & 3) * A_SLOT;
    if constexpr (C < 9) dma16<128 * P>(m0w4 + slot + P * 896, voffWq, rWq, C * 64 * 8192);
    else if constexpr (C < 17) {
      constexpr int i = C - 9;
      constexpr bool isK = (P < 2 && !(i == 7 && P == 1)) || (i == 0 && P == 2);
      constexpr int u = (i == 0 && P == 2) ? 0 : (i == 7 && P == 1) ? 15 : (P < 2 ? 2 * i + 1 + P : 2 * i - 3 + P);
      if constexpr (isK) dma16<0>(m0w1 + slot + P * 8192, voffK, rK, kvbase + u * 8192);
      else               dma16<0>(m0w1 + slot + P * 8192, voffV, rV, kvbase + u * 8192);
    } else if constexpr (C < 26) dma16<0>(m0w4 + slot + P * 1024, voffWo, rWo, (C - 17) * 128 + P * 8 * 1152);
  };
  u32x4 X[NKS];
  auto issueX = [&](auto Cc, auto Pc) AI {
    constexpr int C = decltype(Cc)::value, P = decltype(Pc)::value;
    if constexpr (C < 9) gld16<1024 * P>(X[4 * C + P], voffX, rX, C * 4096);
  };
  {
    const rsrc_t rM = mkrsrc(modp, 2 * 3456 * 4);
    const unsigned vm = (tid < 144 ? tid : 0) * 16;
    u32x4 m0, m1, m2, m3;
    gld16<0>(m0, vm, rM, 0); gld16<0>(m1, vm, rM, 3456 * 4); gld16<D_ * 4>(m2, vm, rM, 0); gld16<D_ * 4>(m3, vm, rM, 3456 * 4);
    sfor<0, 3>([&](auto C) AI { sfor<0, 4>([&](auto P) AI { issueX(C, P); }); sfor<0, 4>([&](auto P) AI { issue(C, P); }); });
    asm volatile("s_waitcnt vmcnt(24)" : "+v"(m0), "+v"(m1), "+v"(m2), "+v"(m3) :: "memory");
    if (tid < 144) {
      f32x4 cv, sv;
#pragma unroll
      for (int e = 0; e < 4; ++e) {
        sv[e] = bfr(__uint_as_float(m0[e]) + __uint_as_float(m1[e])) * QSCALE;
        cv[e] = bfr(1.f + bfr(__uint_as_float(m2[e]) + __uint_as_float(m3[e]))) * QSCALE; }
      const int ks = tid >> 2, a = (tid >> 1) & 1, hh = tid & 1;
      *(LAS f32x4*)(L + A_SPARE + (ks * 2 + hh) * 64 + a * 16) = cv;
      *(LAS f32x4*)(L + A_SPARE + (ks * 2 + hh) * 64 + 32 + a * 16) = sv;
    }
  }

  const int trb = h * 256 + q4 * 64 + blk * 32;
  unsigned aWq = lds0 + trb + (((cp & 1) << 1) | (cp >> 1)) * 8 + hd * 2048, aCS = lds0 + A_SPARE + h * 64;
  PIN(aWq); PIN(aCS);
  AttnState st;
  {
    f32x16 acc[4];
#pragma unroll
    for (int t = 0; t < 4; ++t) acc[t] = f32x16{};
    u32x4 Hf[2]; f32x4 cs[4]; float xf[8];
    auto stage = [&](auto Nc) AI {
      constexpr int n = decltype(Nc)::value, ks = n >> 2, T = n & 3, C = n >> 4, ksl = ks & 3;
      if constexpr ((n & 15) == 0) {
        if constexpr (C > 0) bump(aWq, ((C & 3) - ((C - 1) & 3)) * A_SLOT);
        constexpr int NV = (C + 1 < 9 ? 8 : 4) + (C + 2 < 9 ? 8 : 4);
        asm volatile("s_waitcnt vmcnt(%4) lgkmcnt(0)\n\ts_barrier" : "+v"(X[4 * C]), "+v"(X[4 * C + 1]), "+v"(X[4 * C + 2]), "+v"(X[4 * C + 3]) : "i"(NV) : "memory");
      }
      if constexpr (T == 0) {
#pragma unroll
        for (int i = 0; i < 4; ++i) cs[i] = *(LAS f32x4*)(lp(aCS) + ks * 128 + i * 16);
      }
      st.F[n & 7] = trfrag(lp(aWq) + ((2 * ksl) * 8 + T) * 512, lp(aWq) + ((2 * ksl + 1) * 8 + T) * 512);
      if constexpr ((n & 15) < 8) { if constexpr (n & 1) issue(std::integral_constant<int, C + 3>{}, std::integral_constant<int, ((n & 15) >> 1)>{});
                                    else issueX(std::integral_constant<int, C + 3>{}, std::integral_constant<int, ((n & 15) >> 1)>{}); }
      if constexpr (T == 0) {
#pragma unroll
        for (int e = 0; e < 4; ++e) { xf[2 * e] = bflo(X[ks][e]); xf[2 * e + 1] = bfhi(X[ks][e]); }
#pragma unroll
        for (int e = 0; e < 8; ++e) PIN(xf[e]);
      } else if constexpr (T == 1) {
#pragma unroll
        for (int e = 0; e < 4; ++e) { xf[e] = __builtin_fmaf(xf[e], cs[0][e], cs[2][e]); PIN(xf[e]); }
      } else if constexpr (T == 2) {
#pragma unroll
        for (int e = 0; e < 4; ++e) { xf[4 + e] = __builtin_fmaf(xf[4 + e], cs[1][e], cs[3][e]); PIN(xf[4 + e]); }
      } else {
#pragma unroll
        for (int e = 0; e < 4; ++e) Hf[ks & 1][e] = cvtpk(xf[2 * e], xf[2 * e + 1]);
        PIN(Hf[ks & 1]);
      }
    };
    sfor<0, 4>([&](auto N) AI { stage(N); SBAR(); });
    STAMP(1);
    sfor<0, 144>([&](auto Mc) AI {
      constexpr int m = decltype(Mc)::value;
      if constexpr (m + 4 < 144) stage(std::integral_constant<int, m + 4>{});
      acc[m & 3] = MFMA(st.F[m & 7], asfrag(Hf[(m >> 2) & 1]), acc[m & 3]);
      SBAR();
    });
#pragma unroll
    for (int t = 0; t < 4; ++t) { st.Q[2 * t] = asfrag(pack8(acc[t], 0)); st.Q[2 * t + 1] = asfrag(pack8(acc[t], 1)); }
  }
  STAMP(2);
  unsigned aK0 = lds0 + (9 & 3) * A_SLOT + 2048 * (r32 >> 3) + 64 * (r32 & 7) + 16 * ((0 + h) ^ ((r32 >> 2) & 3));
  unsigned aK1 = aK0 ^ 32, aV = lds0 + (9 & 3) * A_SLOT + trb + cp * 8;
  PIN(aK0); PIN(aK1); PIN(aV);
#pragma unroll
  for (int t = 0; t < 4; ++t) st.O[t] = f32x16{};
  auto kfrag = [&](int off, auto Ic) AI { constexpr int i = decltype(Ic)::value; return ldfrag(lp((i & 1) ? aK1 : aK0) + off + 512 * (i >> 1)); };
  auto vfrag = [&](int off, auto Ic) AI { constexpr int i = decltype(Ic)::value, ks = i >> 2, T = i & 3;
    return trfrag(lp(aV) + off + ((2 * ks) * 4 + T) * 512, lp(aV) + off + ((2 * ks + 1) * 4 + T) * 512); };
  auto step = [&](auto Par, auto HQK, auto HPV, auto NXT, auto NVc, int u, int ko, int vo, int kn, int dsl, auto&& dmaf) AI {
    constexpr int par = decltype(Par)::value, nxt = decltype(NXT)::value, NV = decltype(NVc)::value;
    constexpr bool hqk = decltype(HQK)::value, hpv = decltype(HPV)::value;
    float l = st.l, eprev = 0.f;
    sfor<0, 16>([&](auto Mc) AI {
      constexpr int m = decltype(Mc)::value, n = m + 4;
      if constexpr (n < 8) { if constexpr (hqk) st.F[n & 7] = kfrag(ko, std::integral_constant<int, n>{}); }
      else if constexpr (n < 16) { if constexpr (hpv) st.F[n & 7] = vfrag(vo, std::integral_constant<int, n - 8>{}); }
      else {
        if constexpr (n == 16 && NV >= 0) { bump(aK0, dsl); bump(aK1, dsl); bump(aV, dsl); wait_bar<NV>(); }
        if constexpr (nxt == 1) st.F[n & 7] = kfrag(kn, std::integral_constant<int, n - 16>{});
      }
      dmaf(Mc);
      if constexpr (m < 8) { if constexpr (hqk) st.S[par ^ 1] = MFMA(st.F[m & 7], st.Q[m], m == 0 ? st.negm : st.S[par ^ 1]); }
      else { if constexpr (hpv) st.O[m & 3] = MFMA(st.F[m & 7], asfrag(st.Pk[par ^ 1][(m - 8) >> 2]), st.O[m & 3]); }
      const float e = __builtin_amdgcn_exp2f(st.S[par][m]);
      l += e; PIN(l);
      if constexpr (m & 1) { st.Pk[par][m >> 3][(m >> 1) & 3] = cvtpk(eprev, e); PIN(st.Pk[par][m >> 3]); }
      else { eprev = e; PIN(eprev); }
      SBAR();
    });
    st.l = l;
  };
  using I0 = std::integral_constant<int, 0>; using I1 = std::integral_constant<int, 1>; using IM = std::integral_constant<int, -1>;
  using BT = std::true_type; using BF = std::false_type;
  {
    wait_bar<8>();
    sfor<0, 4>([&](auto P) AI { issue(std::integral_constant<int, 12>{}, P); });
    f32x16 s = f32x16{};
    sfor<0, 8>([&](auto I) AI { s = MFMA(kfrag(2 * 8192, I), st.Q[decltype(I)::value], s); });
    sfor<0, 4>([&](auto I) AI { st.F[decltype(I)::value] = kfrag(0, I); });
    float mt = s[0];
#pragma unroll
    for (int r = 1; r < 16; ++r) mt = __builtin_fmaxf(mt, s[r]);
    mt = swap_max(mt);
#pragma unroll
    for (int r = 0; r < 16; ++r) { st.S[0][r] = s[r] - mt; st.negm[r] = -mt; }
    PIN(st.negm);
    st.l = 0.f;
    SBAR();
  }
  auto nodma = [&](auto) AI {};
  sfor<0, 8>([&](auto Ic) AI {
    constexpr int i = decltype(Ic)::value, C = 9 + i, dsl = (((C + 1) & 3) - (C & 3)) * A_SLOT;
    auto dmaf = [&](auto Mc) AI { constexpr int m = decltype(Mc)::value;
      if constexpr (i > 0 && (m & 3) == 1) issue(std::integral_constant<int, C + 3>{}, std::integral_constant<int, (m >> 2)>{}); };
    if constexpr (i == 0) step(I0{}, BT{}, BF{}, I1{}, IM{}, 0, 0, 0, 8192, 0, dmaf);
    else if constexpr (i < 7) step(I0{}, BT{}, BT{}, I1{}, IM{}, 2 * i, 0, 2 * 8192, 8192, 0, dmaf);
    else step(I0{}, BT{}, BT{}, I0{}, IM{}, 2 * i, 0, 2 * 8192, 0, 0, dmaf);
    if constexpr (i < 7) step(I1{}, BT{}, BT{}, I1{}, std::integral_constant<int, 8>{}, 2 * i + 1, 8192, 3 * 8192, 0, dsl, nodma);
    else step(I1{}, BF{}, BT{}, I0{}, IM{}, 15, 0, 3 * 8192, 0, 0, nodma);
  });
  bf16x8 Of[16];
  {
    constexpr int s16 = (16 & 3) * A_SLOT;
    sfor<0, 8>([&](auto Ic) AI { constexpr int i = decltype(Ic)::value; st.O[i & 3] = MFMA(vfrag(8192, Ic), asfrag(st.Pk[1][i >> 2]), st.O[i & 3]); });
    STAMP(3);
    asm volatile("s_waitcnt lgkmcnt(0)\n\ts_barrier" ::: "memory");
    if (__builtin_expect(__builtin_amdgcn_ballot_w64(!(st.l <= L_LIMIT)) != 0, 0)) {
      const u16* kg = kc + ((size_t)j * SK + (SK - WIN) + r32) * DH + 8 * h;
      const u16* vg = vc + ((size_t)j * SK + (SK - WIN) + 4 * h) * DH + r32;
      float mt = -__builtin_inff();
#pragma unroll 1
      for (int u = 0; u < 16; ++u) {
        f32x16 s = f32x16{};
#pragma unroll
        for (int i = 0; i < 8; ++i) s = MFMA(*(const bf16x8*)(kg + (size_t)u * 32 * DH + 16 * i), st.Q[i], s);
#pragma unroll
        for (int r = 0; r < 16; ++r) mt = __builtin_fmaxf(mt, s[r]);
      }
      mt = swap_max(mt);
      float l = 0.f;
#pragma unroll
      for (int t = 0; t < 4; ++t) st.O[t] = f32x16{};
#pragma unroll 1
      for (int u = 0; u < 16; ++u) {
        f32x16 s = f32x16{};
#pragma unroll
        for (int i = 0; i < 8; ++i) s = MFMA(*(const bf16x8*)(kg + (size_t)u * 32 * DH + 16 * i), st.Q[i], s);
#pragma unroll
        for (int r = 0; r < 16; ++r) { s[r] = __builtin_amdgcn_exp2f(s[r] - mt); l += s[r]; }
#pragma unroll
        for (int ks = 0; ks < 2; ++ks) {
          const bf16x8 pf = asfrag(pack8(s, ks));
#pragma unroll
          for (int t = 0; t < 4; ++t) {
            bf16x8 vf;
#pragma unroll
            for (int e = 0; e < 8; ++e) vf[e] = (short)vg[(size_t)(u * 32 + 16 * ks + 8 * (e >> 2) + (e & 3)) * DH + 32 * t];
            st.O[t] = MFMA(vf, pf, st.O[t]);
          }
        }
      }
      st.l = l;
    }
    const float inv = __builtin_amdgcn_rcpf(swap_add(st.l));
#pragma unroll
    for (int t = 0; t < 4; ++t) {
#pragma unroll
      for (int r = 0; r < 16; ++r) st.O[t][r] *= inv;
      Of[2 * t] = asfrag(pack8(st.O[t], 0)); Of[2 * t + 1] = asfrag(pack8(st.O[t], 1));
    }
    const lptr xw = L + (hd ? s16 : A_SPARE) + g * 8192 + lane * 16, xr = L + (hd ? A_SPARE : s16) + g * 8192 + lane * 16;
#pragma unroll
    for (int i = 0; i < 8; ++i) *(LAS bf16x8*)(xw + i * 1024) = Of[i];
    wait_bar<8>();
#pragma unroll
    for (int i = 0; i < 8; ++i) Of[8 + i] = ldfrag(xr + i * 1024);
    asm volatile("s_waitcnt lgkmcnt(0)\n\ts_barrier" ::: "memory");
    SBAR();
  }
  STAMP(4);
  {
    unsigned aOwn = lds0 + (17 & 3) * A_SLOT + trb + cp * 8 + hd * 512 + hd * 16384, aPar = aOwn ^ 16384;
    PIN(aOwn); PIN(aPar);
    u16* pw = part + ((size_t)(j * NRG + rg) * NKS + 2 * hd) * 512 + lane * 8;
    f32x16 acc[2];
    auto stage = [&](auto Nc) AI {
      constexpr int n = decltype(Nc)::value, i = n >> 4, ks = n & 15, C = 17 + i;
      if constexpr (ks == 0 && i > 0) {
        bump(aOwn, ((C & 3) - ((C - 1) & 3)) * A_SLOT); bump(aPar, ((C & 3) - ((C - 1) & 3)) * A_SLOT);
        constexpr int NV = (C + 1 < 26 ? 4 : 0) + (C + 2 < 26 ? 4 : 0);
        wait_bar<NV>();
      }
      const lptr b = lp(ks < 8 ? aOwn : aPar) + (2 * (ks & 7)) * 1024;
      st.F[n & 7] = trfrag(b, b + 1024);
      if constexpr ((ks & 3) == 1) issue(std::integral_constant<int, C + 3>{}, std::integral_constant<int, (ks >> 2)>{});
    };
    sfor<0, 4>([&](auto N) AI { stage(N); SBAR(); });
    sfor<0, 144>([&](auto Mc) AI {
      constexpr int m = decltype(Mc)::value, i = m >> 4, ks = m & 15;
      if constexpr (m + 4 < 144) stage(std::integral_constant<int, m + 4>{});
      if constexpr (ks == 0) acc[i & 1] = MFMA(st.F[m & 7], Of[0], f32x16{});
      else acc[i & 1] = MFMA(st.F[m & 7], Of[ks], acc[i & 1]);
      if constexpr (i > 0 && (ks == 3 || ks == 5)) { constexpr int s = (ks - 3) >> 1; *(u32x4*)(pw + ((i - 1) * 4 + s) * 512) = pack8(acc[(i - 1) & 1], s); }
      SBAR();
    });
    *(u32x4*)(pw + (8 * 4 + 0) * 512) = pack8(acc[0], 0);
    *(u32x4*)(pw + (8 * 4 + 1) * 512) = pack8(acc[0], 1);
  }
  STAMP(5);
}

constexpr int B_SLOT = 36864, B_LDS = 4 * B_SLOT;

__global__ __launch_bounds__(512, 2) void k3_ffn(const u16* __restrict__ h2, const u16* __restrict__ wg, const u16* __restrict__ wu,
                                                 const u16* __restrict__ wd, u16* __restrict__ part, unsigned long long* dbg) {
  extern __shared__ __attribute__((aligned(16))) char smem[];
  const lptr L = (lptr)smem;
  const unsigned lds0 = (unsigned)(uintptr_t)L;
  const int tid = threadIdx.x, lane = tid & 63;
  const unsigned w = rfl(tid >> 6), g = w & 3, hf = w >> 2;
  const unsigned j = 2 * (blockIdx.x & 7) + ((blockIdx.x >> 3) & 1), rb = blockIdx.x >> 4, rg = rb * 4 + g;
  const int h = lane >> 5, r8 = (lane >> 2) & 7, cp = lane & 3, blk = (lane >> 4) & 1, q4 = (lane >> 2) & 3;
  STAMP(8);
  const rsrc_t rGU = mkrsrc(hf ? wu : wg, D_ * F_ * 2), rWd = mkrsrc(wd, F_ * D_ * 2), rX = mkrsrc(h2, S_ * D_ * 2);
  unsigned voffGU[5];
#pragma unroll
  for (int p = 0; p < 5; ++p) {
    const int piece = p < 4 ? (int)g + 4 * p : 16 + (int)(g & 1), stl = 2 * piece + h, R = stl / 9, sub = stl - 9 * R;
    voffGU[p] = (8 * R + r8) * (F_ * 2) + 576 * j + sub * 64 + ((cp ^ (hf ? 2 : 0)) * 16);
  }
  const unsigned voffWd = (288 * j + 8 * w + r8) * 1152 + h * 64 + cp * 16;
  const unsigned voffWdx = (288 * j + 8 * (32 + g) + r8) * 1152 + h * 64 + cp * 16;
  const unsigned voffX = lane * 16 + rg * (NKS * 1024);
  const unsigned m0gu = lds0 + hf * 18432 + g * 1024, m0gux = lds0 + hf * 18432 + (16 + (g & 1)) * 1024, m0d = lds0 + w * 1024, m0dx = lds0 + (32 + g) * 1024;
  const bool xgu = (g >> 1) != 0, xd = hf != 0;
  auto issue = [&](auto Cc, auto Pc) AI {
    constexpr int C = decltype(Cc)::value, P = decltype(Pc)::value, slot = (C & 3) * B_SLOT;
    if constexpr (C < 18) {
      if constexpr (P < 4) dma16<0>(m0gu + slot + P * 4096, voffGU[P], rGU, C * 32 * F_ * 2);
      else { if (xgu == (bool)(C & 1)) dma16<0>(m0gux + slot, voffGU[4], rGU, C * 32 * F_ * 2); }
    } else if constexpr (C < 27) {
      if constexpr (P < 4) dma16<0>(m0d + slot + P * 8192, voffWd, rWd, (C - 18) * 128 + P * 64 * 1152);
      else { if (xgu == (bool)(C & 1)) {
          const unsigned e = (g & 1) + 2 * hf;
          dma16<0>(lds0 + (32 + e) * 1024 + slot, voffWd + ((32 + e) - w) * 8 * 1152, rWd, (C - 18) * 128); } }
    }
  };
  (void)voffWdx; (void)m0dx; (void)xd;
  u32x4 X[NKS];
  auto issueX = [&](auto Cc, auto Pc) AI {
    constexpr int C = decltype(Cc)::value, P = decltype(Pc)::value;
    if constexpr (C < 18) gld16<1024 * P>(X[2 * C + P], voffX, rX, C * 2048);
  };
  sfor<0, 3>([&](auto C) AI { sfor<0, 2>([&](auto P) AI { issueX(C, P); }); sfor<0, 5>([&](auto P) AI { issue(C, P); }); });

  const int trb = h * 256 + q4 * 64 + cp * 8;
  unsigned aE = lds0 + trb + blk * 18432 + hf * 2048 + ((hf ^ blk) * 32);
  unsigned aO = lds0 + trb + blk * 18432 + hf * 2560 + ((1 ^ hf ^ blk) * 32);
  PIN(aE); PIN(aO);
  bf16x8 F[8], Af[18];
  {
    f32x16 acc[9];
#pragma unroll
    for (int t = 0; t < 9; ++t) acc[t] = f32x16{};
    auto stage = [&](auto Nc) AI {
      constexpr int n = decltype(Nc)::value, ks = n / 9, T = n % 9, C = n / 18, r = n % 18, ksl = ks & 1;
      if constexpr (r == 0) {
        if constexpr (C > 0) { bump(aE, ((C & 3) - ((C - 1) & 3)) * B_SLOT); bump(aO, ((C & 3) - ((C - 1) & 3)) * B_SLOT); }
        constexpr int NV = 9 + (C + 1 < 18 ? 2 : 0) + (C + 2 < 18 ? 2 : 0);
        asm volatile("s_waitcnt vmcnt(%2) lgkmcnt(0)\n\ts_barrier" : "+v"(X[2 * C]), "+v"(X[2 * C + 1]) : "i"(NV) : "memory");
      }
      const lptr b = lp((T & 1) ? aO : aE) + ((2 * ksl) * 9 + (T >> 1)) * 512;
      F[n & 7] = trfrag(b, b + 9 * 512);
      if constexpr (C + 3 != 20) {
        if constexpr (r == 0 || r == 2) issueX(std::integral_constant<int, C + 3>{}, std::integral_constant<int, (r >> 1)>{});
        if constexpr (r >= 4 && r < 14 && (r & 1) == 0) issue(std::integral_constant<int, C + 3>{}, std::integral_constant<int, ((r - 4) >> 1)>{});
      }
    };
    sfor<0, 4>([&](auto N) AI { stage(N); SBAR(); });
    STAMP(9);
    sfor<0, 324>([&](auto Mc) AI {
      constexpr int m = decltype(Mc)::value;
      if constexpr (m + 4 < 324) stage(std::integral_constant<int, m + 4>{});
      acc[m % 9] = MFMA(F[m & 7], asfrag(X[m / 9]), acc[m % 9]);
      SBAR();
    });
    STAMP(10);
    asm volatile("s_waitcnt lgkmcnt(0)\n\ts_barrier" ::: "memory");
    const lptr xw = L + w * 9216 + lane * 16, xr = L + (w ^ 4) * 9216 + lane * 16;
#pragma unroll
    for (int t = 0; t < 9; ++t) {
      float a[8];
#pragma unroll
      for (int e = 0; e < 8; ++e) {
        const float gv = acc[t][e];
        a[e] = gv * acc[t][8 + e] * __builtin_amdgcn_rcpf(1.f + __builtin_amdgcn_exp2f(gv * -1.4426950408889634f));
      }
      u32x4 pk = {cvtpk(a[0], a[1]), cvtpk(a[2], a[3]), cvtpk(a[4], a[5]), cvtpk(a[6], a[7])};
      Af[t] = asfrag(pk);
      *(LAS bf16x8*)(xw + t * 1024) = Af[t];
    }
    wait_bar<0>();
#pragma unroll
    for (int t = 0; t < 9; ++t) Af[9 + t] = ldfrag(xr + t * 1024);
    asm volatile("s_waitcnt lgkmcnt(0)\n\ts_barrier" ::: "memory");
    SBAR();
    sfor<0, 5>([&](auto P) AI { issue(std::integral_constant<int, 20>{}, P); });
  }
  STAMP(11);
  {
    const unsigned aT = lds0 + (18 & 3) * B_SLOT + trb + blk * 32 + hf * 512;
    unsigned aOwn = aT + hf * 18432, aPar = aT + (1 - hf) * 18432;
    PIN(aOwn); PIN(aPar);
    u16* pw = part + ((size_t)(j * NRG + rg) * NKS + 2 * hf) * 512 + lane * 8;
    f32x16 acc[2];
    auto stage = [&](auto Nc) AI {
      constexpr int n = decltype(Nc)::value, i = n / 18, ks = n % 18, C = 18 + i;
      if constexpr (ks == 0 && i > 0) {
        bump(aOwn, ((C & 3) - ((C - 1) & 3)) * B_SLOT); bump(aPar, ((C & 3) - ((C - 1) & 3)) * B_SLOT);
        constexpr int NV = C + 2 < 27 ? 9 : (C + 1 < 27 ? 4 : 0);
        wait_bar<NV>();
      }
      const lptr b = lp(ks < 9 ? aOwn : aPar) + (2 * (ks % 9)) * 1024;
      F[n & 7] = trfrag(b, b + 1024);
      if constexpr (ks >= 1 && ks < 16 && (ks % 3) == 1) issue(std::integral_constant<int, C + 3>{}, std::integral_constant<int, (ks / 3)>{});
    };
    sfor<0, 4>([&](auto N) AI { stage(N); SBAR(); });
    sfor<0, 162>([&](auto Mc) AI {
      constexpr int m = decltype(Mc)::value, i = m / 18, ks = m % 18;
      if constexpr (m + 4 < 162) stage(std::integral_constant<int, m + 4>{});
      if constexpr (ks == 0) acc[i & 1] = MFMA(F[m & 7], Af[0], f32x16{});
      else acc[i & 1] = MFMA(F[m & 7], Af[ks], acc[i & 1]);
      if constexpr (i > 0 && (ks == 3 || ks == 5)) { constexpr int s = (ks - 3) >> 1; *(u32x4*)(pw + ((i - 1) * 4 + s) * 512) = pack8(acc[(i - 1) & 1], s); }
      SBAR();
    });
    *(u32x4*)(pw + (8 * 4 + 0) * 512) = pack8(acc[0], 0);
    *(u32x4*)(pw + (8 * 4 + 1) * 512) = pack8(acc[0], 1);
  }
  STAMP(12);
}

extern "C" void kernel_launch(void* const* d_in, const int* in_sizes, int n_in, void* d_out, int out_size, void* d_ws, size_t ws_size,
                              hipStream_t stream) {
  (void)in_sizes; (void)n_in; (void)out_size; (void)ws_size;
  const u16* x = (const u16*)d_in[0]; const u16* kc = (const u16*)d_in[1]; const u16* vc = (const u16*)d_in[2];
  const u16* wq = (const u16*)d_in[3]; const u16* wo = (const u16*)d_in[4];
  const u16* wg = (const u16*)d_in[6]; const u16* wu = (const u16*)d_in[7]; const u16* wd = (const u16*)d_in[8];
  const u16* c = (const u16*)d_in[9]; const u16* wmod = (const u16*)d_in[10];
  char* ws = (char*)d_ws;
  float* modp = (float*)(ws + WS_MODP); u16* xh = (u16*)(ws + WS_XH); u16* h2 = (u16*)(ws + WS_H2); float* x2 = (float*)(ws + WS_X2);
  u16* p1 = (u16*)(ws + WS_P); u16* p2 = (u16*)(ws + WS_P2); unsigned long long* dbg = (unsigned long long*)(ws + WS_DBG);
  (void)hipFuncSetAttribute((const void*)k1_attn, hipFuncAttributeMaxDynamicSharedMemorySize, A_LDS);
  (void)hipFuncSetAttribute((const void*)k3_ffn, hipFuncAttributeMaxDynamicSharedMemorySize, B_LDS);
  k0_prep<<<108 + 256, 576, 0, stream>>>(x, c, wmod, modp, xh);
  k1_attn<<<256, 512, A_LDS, stream>>>(xh, wq, kc, vc, wo, modp, p1, dbg);
  k_reduce<false><<<256, 576, 0, stream>>>(p1, x, modp, x2, h2, (u16*)d_out);
  k3_ffn<<<256, 512, B_LDS, stream>>>(h2, wg, wu, wd, p2, dbg);
  k_reduce<true><<<256, 576, 0, stream>>>(p2, x, modp, x2, h2, (u16*)d_out);
}
```

```cpp
#include <hip/hip_runtime.h>
#include <stdint.h>
#include <type_traits>

typedef short    bf16x8 __attribute__((ext_vector_type(8)));
typedef short    s16x4  __attribute__((ext_vector_type(4)));
typedef float    f32x16 __attribute__((ext_vector_type(16)));
typedef float    f32x4  __attribute__((ext_vector_type(4)));
typedef float    f32x2  __attribute__((ext_vector_type(2)));
typedef __bf16   bf16x2 __attribute__((ext_vector_type(2)));
typedef unsigned u32x4  __attribute__((ext_vector_type(4)));
typedef unsigned u32x2  __attribute__((ext_vector_type(2)));
typedef unsigned short u16;
#define LAS __attribute__((address_space(3)))
typedef LAS char* lptr;
typedef __amdgpu_buffer_rsrc_t rsrc_t;
#define DI __device__ __forceinline__
#define AI __attribute__((always_inline))
#define SBAR() __builtin_amdgcn_sched_barrier(0)
#define PIN(x) asm volatile("" : "+v"(x))
#define MFMA(a, b, c) __builtin_amdgcn_mfma_f32_32x32x16_bf16((a), (b), (c), 0, 0, 0)

constexpr int S_ = 2048, D_ = 576, HQ = 32, HKV = 16, SK = 2048, DH = 128, WIN = 512, F_ = 4608;
constexpr int NKS = D_ / 16;
constexpr int NRG = S_ / 32;
constexpr float LN_EPS = 1e-5f;
constexpr float QSCALE = 0.08838834764831845f * 1.4426950408889634f;

constexpr size_t WS_CNT  = 0;
constexpr size_t WS_TABA = 16384;
constexpr size_t WS_TABB = 24576;
constexpr size_t WS_XH   = 32768;
constexpr size_t ACT_B   = (size_t)S_ * D_ * 2;
constexpr size_t WS_H2   = WS_XH + ACT_B;
constexpr size_t WS_X2   = WS_H2 + ACT_B;
constexpr size_t WS_P    = WS_X2 + 2 * ACT_B;
constexpr size_t WS_P2   = WS_P;
constexpr size_t WS_DBG  = WS_P + 32 * ACT_B;

DI unsigned rfl(unsigned x) { return __builtin_amdgcn_readfirstlane(x); }
DI unsigned cvtpk(float lo, float hi) { f32x2 v = {lo, hi}; bf16x2 b = __builtin_convertvector(v, bf16x2); return __builtin_bit_cast(unsigned, b); }
DI float bflo(unsigned w) { return __uint_as_float(w << 16); }
DI float bfhi(unsigned w) { return __uint_as_float(w & 0xffff0000u); }
DI float bfr(float x) { return bflo(cvtpk(x, x)); }
DI rsrc_t mkrsrc(const void* p, unsigned bytes) { return __builtin_amdgcn_make_buffer_rsrc((void*)p, (short)0, (int)bytes, 0x00020000); }
template <int OFF> DI void dma16(unsigned m0v, unsigned voff, rsrc_t r, unsigned soff) {
  asm volatile("s_mov_b32 m0, %0\n\ts_nop 0\n\tbuffer_load_dwordx4 %1, %2, %3 offen offset:%4 lds"
               :: "s"(m0v), "v"(voff), "s"(r), "s"(soff), "i"(OFF) : "memory");
}
DI void touch(unsigned m0v, unsigned voff, rsrc_t r) {
  asm volatile("s_mov_b32 m0, %0\n\ts_nop 0\n\tbuffer_load_dword %1, %2, 0 offen lds" :: "s"(m0v), "v"(voff), "s"(r) : "memory");
}
template <int OFF> DI void gld16(u32x4& d, unsigned voff, rsrc_t r, unsigned soff) {
  asm volatile("buffer_load_dwordx4 %0, %1, %2, %3 offen offset:%4" : "=v"(d) : "v"(voff), "s"(r), "s"(soff), "i"(OFF) : "memory");
}
template <int OFF> DI void gld16c(u32x4& d, unsigned voff, rsrc_t r, unsigned soff) {
  asm volatile("buffer_load_dwordx4 %0, %1, %2, %3 offen offset:%4 sc1" : "=v"(d) : "v"(voff), "s"(r), "s"(soff), "i"(OFF) : "memory");
}
DI s16x4 trd(lptr p) { return __builtin_amdgcn_ds_read_tr16_b64_v4i16((LAS s16x4*)p); }
DI bf16x8 trfrag(lptr p0, lptr p1) { s16x4 lo = trd(p0), hi = trd(p1); return __builtin_shufflevector(lo, hi, 0, 1, 2, 3, 4, 5, 6, 7); }
DI bf16x8 ldfrag(lptr p) { return *(LAS bf16x8*)p; }
DI bf16x8 asfrag(u32x4 w) { return __builtin_bit_cast(bf16x8, w); }
DI float swap_max(float m) { auto rr = __builtin_amdgcn_permlane32_swap(__float_as_uint(m), __float_as_uint(m), false, false);
  return __builtin_fmaxf(__uint_as_float(rr[0]), __uint_as_float(rr[1])); }
DI float swap_add(float m) { auto rr = __builtin_amdgcn_permlane32_swap(__float_as_uint(m), __float_as_uint(m), false, false);
  return __uint_as_float(rr[0]) + __uint_as_float(rr[1]); }
DI float swap16_add(float a, float b) { auto rr = __builtin_amdgcn_permlane16_swap(__float_as_uint(a), __float_as_uint(b), false, false);
  return __uint_as_float(rr[0]) + __uint_as_float(rr[1]); }
DI float swap32_add(float a, float b) { auto rr = __builtin_amdgcn_permlane32_swap(__float_as_uint(a), __float_as_uint(b), false, false);
  return __uint_as_float(rr[0]) + __uint_as_float(rr[1]); }
DI float ror8(float a) { return __int_as_float(__builtin_amdgcn_update_dpp(0, __float_as_int(a), 0x128, 0xf, 0xf, false)); }
DI void lbar() { asm volatile("s_waitcnt lgkmcnt(0)\n\ts_barrier" ::: "memory"); }
DI void arrive_and_wait(unsigned* cnt, int tid) {
  if (tid == 0) {
    __hip_atomic_fetch_add(cnt, 1u, __ATOMIC_RELAXED, __HIP_MEMORY_SCOPE_AGENT);
    for (int sp = 0; sp < (1 << 20); ++sp) {
      if (__hip_atomic_load(cnt, __ATOMIC_RELAXED, __HIP_MEMORY_SCOPE_AGENT) >= 16u) break;
      __builtin_amdgcn_s_sleep(1);
    }
  }
  asm volatile("s_barrier" ::: "memory");
}
DI u32x4 pack8(const f32x16& a, int s) {
  u32x4 w = {cvtpk(a[8 * s], a[8 * s + 1]), cvtpk(a[8 * s + 2], a[8 * s + 3]), cvtpk(a[8 * s + 4], a[8 * s + 5]), cvtpk(a[8 * s + 6], a[8 * s + 7])};
  return w;
}
DI lptr lp(unsigned a) { return (lptr)(uintptr_t)a; }
DI void bump(unsigned& a, int d) { a += d; PIN(a); }
template <int N> DI void wait_bar() { asm volatile("s_waitcnt vmcnt(%0) lgkmcnt(0)\n\ts_barrier" :: "i"(N) : "memory"); }
template <int I, int N, class F> DI void sfor(F&& f) { if constexpr (I < N) { f(std::integral_constant<int, I>{}); sfor<I + 1, N>(f); } }

#define STAMP(i)

DI float block_rowsum(float v, float* red, int tid) {
  v += __shfl_xor(v, 8); v += __shfl_xor(v, 16); v += __shfl_xor(v, 32);
  if ((tid & 63) < 8) red[(tid >> 6) * 8 + (tid & 7)] = v;
  __syncthreads();
  float s = 0.f;
#pragma unroll
  for (int i = 0; i < 9; ++i) s += red[i * 8 + (tid & 7)];
  __syncthreads();
  return s;
}

__global__ __launch_bounds__(576) void k0_prep(const u16* __restrict__ x, const u16* __restrict__ c, const u16* __restrict__ wmod,
                                               unsigned* __restrict__ cnt, float* __restrict__ tabA, u16* __restrict__ tabB, u16* __restrict__ xh) {
  __shared__ float red[9 * 64];
  const int tid = threadIdx.x;
  if (blockIdx.x < 108) {
    const int cc = tid & 3, kk = tid >> 2;
    u32x4 w[4]; float sc[4];
#pragma unroll
    for (int i = 0; i < 4; ++i) {
      const int k = kk + 144 * i;
      w[i] = *(const u32x4*)(wmod + (size_t)k * 3456 + blockIdx.x * 32 + cc * 8);
      const float cv = bflo(c[k]);
      sc[i] = bfr(cv / (1.f + __expf(-cv)));
    }
    float a[8];
#pragma unroll
    for (int e = 0; e < 8; ++e) a[e] = 0.f;
#pragma unroll
    for (int i = 0; i < 4; ++i)
#pragma unroll
      for (int e = 0; e < 4; ++e) { a[2 * e] += sc[i] * bflo(w[i][e]); a[2 * e + 1] += sc[i] * bfhi(w[i][e]); }
#pragma unroll
    for (int e = 0; e < 8; ++e) { a[e] += __shfl_xor(a[e], 4); a[e] += __shfl_xor(a[e], 8); a[e] += __shfl_xor(a[e], 16); a[e] += __shfl_xor(a[e], 32); }
    if ((tid & 63) < 4) {
#pragma unroll
      for (int e = 0; e < 8; ++e) red[(tid >> 6) * 32 + cc * 8 + e] = a[e];
    }
    __syncthreads();
    if (tid < 32) {
      float s = 0.f;
#pragma unroll
      for (int i = 0; i < 9; ++i) s += red[i * 32 + tid];
      s = bfr(s);
      const int v = blockIdx.x / 18, n = (blockIdx.x - 18 * v) * 32 + tid;
      if (v == 1 || v == 4) s = bfr(1.f + s);
      if (v < 2) tabA[((n >> 4) * 2 + ((n >> 2) & 1)) * 16 + (v == 0 ? 8 : 0) + ((n >> 3) & 1) * 4 + (n & 3)] = s * QSCALE;
      else tabB[(v - 2) * D_ + n] = (u16)(__float_as_uint(s) >> 16);
    }
    if (blockIdx.x == 0 && tid >= 64 && tid < 128) cnt[(tid - 64) * 64] = 0u;
  } else {
    const int b = blockIdx.x - 108, row = b * 8 + (tid & 7), ci = tid >> 3, ks = ci >> 1, h = ci & 1;
    const u16* xr = x + (size_t)row * D_ + 16 * ks + 4 * h;
    const u32x2 a0 = *(const u32x2*)xr, a1 = *(const u32x2*)(xr + 8);
    float v[8] = {bflo(a0[0]), bfhi(a0[0]), bflo(a0[1]), bfhi(a0[1]), bflo(a1[0]), bfhi(a1[0]), bflo(a1[1]), bfhi(a1[1])};
    float s = 0.f;
#pragma unroll
    for (int e = 0; e < 8; ++e) s += v[e];
    const float mean = block_rowsum(s, red, tid) * (1.f / D_);
    float q = 0.f;
#pragma unroll
    for (int e = 0; e < 8; ++e) { v[e] -= mean; q += v[e] * v[e]; }
    const float rstd = rsqrtf(block_rowsum(q, red, tid) * (1.f / D_) + LN_EPS);
    u32x4 o = {cvtpk(v[0] * rstd, v[1] * rstd), cvtpk(v[2] * rstd, v[3] * rstd), cvtpk(v[4] * rstd, v[5] * rstd), cvtpk(v[6] * rstd, v[7] * rstd)};
    *(u32x4*)(xh + ((size_t)((row >> 5) * NKS + ks) * 64 + h * 32 + (row & 31)) * 8) = o;
  }
}

constexpr int A_SLOT = 32768, A_SPARE = 4 * A_SLOT, A_LDS = 5 * A_SLOT;
constexpr float L_LIMIT = 1.2379400392853803e27f;
constexpr float M_MARGIN = 40.f;

#define WSTEP 128
#define WSTEP_N(b) ((b) / WSTEP)
struct AttnState {
  f32x16 O[4], S[2], negm; u32x4 Pk[2][2]; bf16x8 Q[8], F[8]; float l;
};

constexpr int B_SLOT = 36864, B_SPARE = 4 * B_SLOT, SP_X = B_SPARE + 5120, SP_RED = B_SPARE + 14336;
constexpr int bslot(int C) { return ((C + 2) & 3) * B_SLOT; }

__global__ __launch_bounds__(512, 2) void kmain(const u16* __restrict__ x, const u16* __restrict__ wq, const u16* __restrict__ kc,
                                                const u16* __restrict__ vc, const u16* __restrict__ wo, const u16* __restrict__ wg,
                                                const u16* __restrict__ wu, const u16* __restrict__ wd, char* __restrict__ ws, u16* __restrict__ out) {
  const u16* xh = (const u16*)(ws + WS_XH);
  extern __shared__ __attribute__((aligned(16))) char smem[];
  const lptr L = (lptr)smem;
  const unsigned lds0 = (unsigned)(uintptr_t)L;
  const int tid = threadIdx.x, lane = tid & 63;
  const unsigned w = rfl(tid >> 6), g = w & 3, hd = w >> 2;
  const unsigned j = 2 * (blockIdx.x & 7) + ((blockIdx.x >> 3) & 1), rb = blockIdx.x >> 4, rg = rb * 4 + g;
  const int r32 = lane & 31, h = lane >> 5, r8 = (lane >> 2) & 7, cp = lane & 3, blk = (lane >> 4) & 1, q4 = (lane >> 2) & 3;
  STAMP(0);
  const rsrc_t rWq = mkrsrc(wq, D_ * 4096 * 2), rK = mkrsrc(kc, HKV * SK * DH * 2), rV = mkrsrc(vc, HKV * SK * DH * 2),
               rWo = mkrsrc(wo, 4096 * D_ * 2), rX = mkrsrc(xh, S_ * D_ * 2);
  const unsigned voffWq = (8 * w + r8) * 8192 + h * 64 + cp * 16 + 512 * j;
  const unsigned voffX = lane * 16 + rg * (NKS * 1024);
  const unsigned kvrow = 8 * (w >> 1) + r8;
  const unsigned voffV = kvrow * 256 + (2 * (w & 1) + h) * 64 + cp * 16;
  const unsigned voffK = kvrow * 256 + (2 * (w & 1) + h) * 64 + ((cp ^ ((kvrow >> 2) & 3)) * 16);
  const unsigned kvbase = (j * SK + (SK - WIN)) * 256;
  const unsigned voffWo = (256 * j + 32 * w + r8) * 1152 + h * 64 + cp * 16;
  const unsigned m0w4 = lds0 + w * 4096, m0w1 = lds0 + w * 1024;

  auto issue = [&](auto Cc, auto Pc) AI {
    constexpr int C = decltype(Cc)::value, P = decltype(Pc)::value, slot = (C & 3) * A_SLOT;
    if constexpr (C < 9) dma16<128 * P>(m0w4 + slot + P * 896, voffWq, rWq, C * 64 * 8192);
    else if constexpr (C < 17) {
      constexpr int i = C - 9;
      constexpr bool isK = (P < 2 && !(i == 7 && P == 1)) || (i == 0 && P == 2);
      constexpr int u = (i == 0 && P == 2) ? 0 : (i == 7 && P == 1) ? 15 : (P < 2 ? 2 * i + 1 + P : 2 * i - 3 + P);
      if constexpr (isK) dma16<0>(m0w1 + slot + P * 8192, voffK, rK, kvbase + u * 8192);
      else               dma16<0>(m0w1 + slot + P * 8192, voffV, rV, kvbase + u * 8192);
    } else if constexpr (C < 26) dma16<0>(m0w4 + slot + P * 1024, voffWo, rWo, (C - 17) * 128 + P * 8 * 1152);
  };
  u32x4 X[NKS];
  auto issueX = [&](auto Cc, auto Pc) AI {
    constexpr int C = decltype(Cc)::value, P = decltype(Pc)::value;
    if constexpr (C < 9) gld16<1024 * P>(X[4 * C + P], voffX, rX, C * 4096);
  };
  dma16<0>(m0w1 + A_SPARE, lane * 16 + w * 1024, mkrsrc(ws + WS_TABA, 4608), 0);
  sfor<0, 3>([&](auto C) AI { sfor<0, 4>([&](auto P) AI { issueX(C, P); }); sfor<0, 4>([&](auto P) AI { issue(C, P); }); });

  const int trb = h * 256 + q4 * 64 + blk * 32;
  unsigned aWq = lds0 + trb + (((cp & 1) << 1) | (cp >> 1)) * 8 + hd * 2048, aCS = lds0 + A_SPARE + h * 64;
  PIN(aWq); PIN(aCS);
  AttnState st;
  {
    f32x16 acc[4];
#pragma unroll
    for (int t = 0; t < 4; ++t) acc[t] = f32x16{};
    u32x4 Hf[2]; f32x4 cs[4]; float xf[8];
    auto stage = [&](auto Nc) AI {
      constexpr int n = decltype(Nc)::value, ks = n >> 2, T = n & 3, C = n >> 4, ksl = ks & 3;
      if constexpr ((n & 15) == 0) {
        if constexpr (C > 0) bump(aWq, ((C & 3) - ((C - 1) & 3)) * A_SLOT);
        constexpr int NV = (C + 1 < 9 ? 8 : 4) + (C + 2 < 9 ? 8 : 4);
        asm volatile("s_waitcnt vmcnt(%4) lgkmcnt(0)\n\ts_barrier" : "+v"(X[4 * C]), "+v"(X[4 * C + 1]), "+v"(X[4 * C + 2]), "+v"(X[4 * C + 3]) : "i"(NV) : "memory");
      }
      if constexpr (T == 0) {
#pragma unroll
        for (int i = 0; i < 4; ++i) cs[i] = *(LAS f32x4*)(lp(aCS) + ks * 128 + i * 16);
      }
      st.F[n & 7] = trfrag(lp(aWq) + ((2 * ksl) * 8 + T) * 512, lp(aWq) + ((2 * ksl + 1) * 8 + T) * 512);
      if constexpr ((n & 15) < 8) { if constexpr (n & 1) issue(std::integral_constant<int, C + 3>{}, std::integral_constant<int, ((n & 15) >> 1)>{});
                                    else issueX(std::integral_constant<int, C + 3>{}, std::integral_constant<int, ((n & 15) >> 1)>{}); }
      if constexpr (T == 0) {
#pragma unroll
        for (int e = 0; e < 4; ++e) { xf[2 * e] = bflo(X[ks][e]); xf[2 * e + 1] = bfhi(X[ks][e]); }
#pragma unroll
        for (int e = 0; e < 8; ++e) PIN(xf[e]);
      } else if constexpr (T == 1) {
#pragma unroll
        for (int e = 0; e < 4; ++e) { xf[e] = __builtin_fmaf(xf[e], cs[0][e], cs[2][e]); PIN(xf[e]); }
      } else if constexpr (T == 2) {
#pragma unroll
        for (int e = 0; e < 4; ++e) { xf[4 + e] = __builtin_fmaf(xf[4 + e], cs[1][e], cs[3][e]); PIN(xf[4 + e]); }
      } else {
#pragma unroll
        for (int e = 0; e < 4; ++e) Hf[ks & 1][e] = cvtpk(xf[2 * e], xf[2 * e + 1]);
        PIN(Hf[ks & 1]);
      }
    };
    sfor<0, 4>([&](auto N) AI { stage(N); SBAR(); });
    STAMP(1);
    sfor<0, 144>([&](auto Mc) AI {
      constexpr int m = decltype(Mc)::value;
      if constexpr (m + 4 < 144) stage(std::integral_constant<int, m + 4>{});
      acc[m & 3] = MFMA(st.F[m & 7], asfrag(Hf[(m >> 2) & 1]), acc[m & 3]);
      SBAR();
    });
#pragma unroll
    for (int t = 0; t < 4; ++t) { st.Q[2 * t] = asfrag(pack8(acc[t], 0)); st.Q[2 * t + 1] = asfrag(pack8(acc[t], 1)); }
  }
  STAMP(2);
  unsigned aK0 = lds0 + (9 & 3) * A_SLOT + 2048 * (r32 >> 3) + 64 * (r32 & 7) + 16 * ((0 + h) ^ ((r32 >> 2) & 3));
  unsigned aK1 = aK0 ^ 32, aV = lds0 + (9 & 3) * A_SLOT + trb + cp * 8;
  PIN(aK0); PIN(aK1); PIN(aV);
#pragma unroll
  for (int t = 0; t < 4; ++t) st.O[t] = f32x16{};
  auto kfrag = [&](int off, auto Ic) AI { constexpr int i = decltype(Ic)::value; return ldfrag(lp((i & 1) ? aK1 : aK0) + off + 512 * (i >> 1)); };
  auto vfrag = [&](int off, auto Ic) AI { constexpr int i = decltype(Ic)::value, ks = i >> 2, T = i & 3;
    return trfrag(lp(aV) + off + ((2 * ks) * 4 + T) * 512, lp(aV) + off + ((2 * ks + 1) * 4 + T) * 512); };
  auto step = [&](auto Par, auto HQK, auto HPV, auto NXT, auto NVc, int u, int ko, int vo, int kn, int dsl, auto&& dmaf) AI {
    constexpr int par = decltype(Par)::value, nxt = decltype(NXT)::value, NV = decltype(NVc)::value;
    constexpr bool hqk = decltype(HQK)::value, hpv = decltype(HPV)::value;
    float l = st.l, eprev = 0.f;
    sfor<0, 16>([&](auto Mc) AI {
      constexpr int m = decltype(Mc)::value, n = m + 4;
      if constexpr (n < 8) { if constexpr (hqk) st.F[n & 7] = kfrag(ko, std::integral_constant<int, n>{}); }
      else if constexpr (n < 16) { if constexpr (hpv) st.F[n & 7] = vfrag(vo, std::integral_constant<int, n - 8>{}); }
      else {
        if constexpr (n == 16 && NV >= 0) { bump(aK0, dsl); bump(aK1, dsl); bump(aV, dsl); wait_bar<NV>(); }
        if constexpr (nxt == 1) st.F[n & 7] = kfrag(kn, std::integral_constant<int, n - 16>{});
      }
      dmaf(Mc);
      if constexpr (m < 8) { if constexpr (hqk) st.S[par ^ 1] = MFMA(st.F[m & 7], st.Q[m], m == 0 ? st.negm : st.S[par ^ 1]); }
      else { if constexpr (hpv) st.O[m & 3] = MFMA(st.F[m & 7], asfrag(st.Pk[par ^ 1][(m - 8) >> 2]), st.O[m & 3]); }
      const float e = __builtin_amdgcn_exp2f(st.S[par][m]);
      l += e; PIN(l);
      if constexpr (m & 1) { st.Pk[par][m >> 3][(m >> 1) & 3] = cvtpk(eprev, e); PIN(st.Pk[par][m >> 3]); }
      else { eprev = e; PIN(eprev); }
      SBAR();
    });
    st.l = l;
  };
  using I0 = std::integral_constant<int, 0>; using I1 = std::integral_constant<int, 1>; using IM = std::integral_constant<int, -1>;
  using BT = std::true_type; using BF = std::false_type;
  {
    wait_bar<8>();
    sfor<0, 4>([&](auto P) AI { issue(std::integral_constant<int, 12>{}, P); });
    f32x16 s = f32x16{};
    sfor<0, 8>([&](auto I) AI { s = MFMA(kfrag(2 * 8192, I), st.Q[decltype(I)::value], s); });
    sfor<0, 4>([&](auto I) AI { st.F[decltype(I)::value] = kfrag(0, I); });
    float mt = s[0];
#pragma unroll
    for (int r = 1; r < 16; ++r) mt = __builtin_fmaxf(mt, s[r]);
    mt = swap_max(mt) + M_MARGIN;
#pragma unroll
    for (int r = 0; r < 16; ++r) { st.S[0][r] = s[r] - mt; st.negm[r] = -mt; }
    PIN(st.negm);
    st.l = 0.f;
    SBAR();
  }
  auto nodma = [&](auto) AI {};
  sfor<0, 8>([&](auto Ic) AI {
    constexpr int i = decltype(Ic)::value, C = 9 + i, dsl = (((C + 1) & 3) - (C & 3)) * A_SLOT;
    auto dmaf = [&](auto Mc) AI { constexpr int m = decltype(Mc)::value;
      if constexpr (i > 0 && (m & 3) == 1) issue(std::integral_constant<int, C + 3>{}, std::integral_constant<int, (m >> 2)>{}); };
    if constexpr (i == 0) step(I0{}, BT{}, BF{}, I1{}, IM{}, 0, 0, 0, 8192, 0, dmaf);
    else if constexpr (i < 7) step(I0{}, BT{}, BT{}, I1{}, IM{}, 2 * i, 0, 2 * 8192, 8192, 0, dmaf);
    else step(I0{}, BT{}, BT{}, I0{}, IM{}, 2 * i, 0, 2 * 8192, 0, 0, dmaf);
    auto warm = [&](auto Mc) AI { constexpr int m = decltype(Mc)::value;
      if constexpr (m == 13) {
        unsigned t = tid; PIN(t);
        if constexpr (i == 0) touch(lds0 + A_SPARE, (256 * j + 16 * rb) * 1152 + (t < WSTEP_N(18432) ? t : 0) * WSTEP, rWo);
        if constexpr (i == 1 || (i == 2 && WSTEP == 64)) { const unsigned u = (t & 255) + (i - 1) * 256, nl = 640 / WSTEP, rr = u / nl, cc = u - rr * nl;
          touch(lds0 + A_SPARE, (36 * rb + (rr < 36 ? rr : 0)) * (F_ * 2) + ((576 * j) & ~127u) + cc * WSTEP, mkrsrc(hd ? wu : wg, D_ * F_ * 2)); }
        if constexpr (i == 3) touch(lds0 + A_SPARE, (288 * j + 18 * rb) * 1152 + (t < WSTEP_N(20736) ? t : 0) * WSTEP, mkrsrc(wd, F_ * D_ * 2));
      }
    };
    if constexpr (i < 7) step(I1{}, BT{}, BT{}, I1{}, std::integral_constant<int, 8>{}, 2 * i + 1, 8192, 3 * 8192, 0, dsl, warm);
    else step(I1{}, BF{}, BT{}, I0{}, IM{}, 15, 0, 3 * 8192, 0, 0, nodma);
  });
  bf16x8 Of[16];
  {
    constexpr int s16 = (16 & 3) * A_SLOT;
    sfor<0, 8>([&](auto Ic) AI { constexpr int i = decltype(Ic)::value; st.O[i & 3] = MFMA(vfrag(8192, Ic), asfrag(st.Pk[1][i >> 2]), st.O[i & 3]); });
    STAMP(3);
    asm volatile("s_waitcnt lgkmcnt(0)\n\ts_barrier" ::: "memory");
    if (__builtin_expect(__builtin_amdgcn_ballot_w64(!(st.l <= L_LIMIT)) != 0, 0)) {
      const u16* kg = kc + ((size_t)j * SK + (SK - WIN) + r32) * DH + 8 * h;
      const u16* vg = vc + ((size_t)j * SK + (SK - WIN) + 4 * h) * DH + r32;
      float mt = -__builtin_inff();
#pragma unroll 1
      for (int u = 0; u < 16; ++u) {
        f32x16 s = f32x16{};
#pragma unroll
        for (int i = 0; i < 8; ++i) s = MFMA(*(const bf16x8*)(kg + (size_t)u * 32 * DH + 16 * i), st.Q[i], s);
#pragma unroll
        for (int r = 0; r < 16; ++r) mt = __builtin_fmaxf(mt, s[r]);
      }
      mt = swap_max(mt);
      float l = 0.f;
#pragma unroll
      for (int t = 0; t < 4; ++t) st.O[t] = f32x16{};
#pragma unroll 1
      for (int u = 0; u < 16; ++u) {
        f32x16 s = f32x16{};
#pragma unroll
        for (int i = 0; i < 8; ++i) s = MFMA(*(const bf16x8*)(kg + (size_t)u * 32 * DH + 16 * i), st.Q[i], s);
#pragma unroll
        for (int r = 0; r < 16; ++r) { s[r] = __builtin_amdgcn_exp2f(s[r] - mt); l += s[r]; }
#pragma unroll
        for (int ks = 0; ks < 2; ++ks) {
          const bf16x8 pf = asfrag(pack8(s, ks));
#pragma unroll
          for (int t = 0; t < 4; ++t) {
            bf16x8 vf;
#pragma unroll
            for (int e = 0; e < 8; ++e) vf[e] = (short)vg[(size_t)(u * 32 + 16 * ks + 8 * (e >> 2) + (e & 3)) * DH + 32 * t];
            st.O[t] = MFMA(vf, pf, st.O[t]);
          }
        }
      }
      st.l = l;
    }
    const float inv = __builtin_amdgcn_rcpf(swap_add(st.l));
#pragma unroll
    for (int t = 0; t < 4; ++t) {
#pragma unroll
      for (int r = 0; r < 16; ++r) st.O[t][r] *= inv;
      Of[2 * t] = asfrag(pack8(st.O[t], 0)); Of[2 * t + 1] = asfrag(pack8(st.O[t], 1));
    }
    const lptr xw = L + (hd ? s16 : A_SPARE) + g * 8192 + lane * 16, xr = L + (hd ? A_SPARE : s16) + g * 8192 + lane * 16;
#pragma unroll
    for (int i = 0; i < 8; ++i) *(LAS bf16x8*)(xw + i * 1024) = Of[i];
    wait_bar<8>();
#pragma unroll
    for (int i = 0; i < 8; ++i) Of[8 + i] = ldfrag(xr + i * 1024);
    asm volatile("s_waitcnt lgkmcnt(0)\n\ts_barrier" ::: "memory");
    SBAR();
  }
  STAMP(4);
  const unsigned hf = hd;
  const rsrc_t rGU = mkrsrc(hf ? wu : wg, D_ * F_ * 2), rWd = mkrsrc(wd, F_ * D_ * 2), rH2 = mkrsrc(ws + WS_H2, S_ * D_ * 2);
  unsigned lane2 = lane; PIN(lane2);
  const int h_ = lane2 >> 5, r8_ = (lane2 >> 2) & 7, cp_ = lane2 & 3;
  unsigned voffGU[5];
#pragma unroll
  for (int p = 0; p < 5; ++p) {
    const int piece = p < 4 ? (int)g + 4 * p : 16 + (int)(g & 1), stl = 2 * piece + h_, R = stl / 9, sub = stl - 9 * R;
    voffGU[p] = (8 * R + r8_) * (F_ * 2) + 576 * j + sub * 64 + ((cp_ ^ (hf ? 2 : 0)) * 16);
  }
  const unsigned m0gu = lds0 + hf * 18432 + g * 1024, m0gux = lds0 + hf * 18432 + (16 + (g & 1)) * 1024;
  const bool xgu = (g >> 1) != 0;
  unsigned voffWd = 0;
  auto issueF = [&](auto Cc, auto Pc) AI {
    constexpr int C = decltype(Cc)::value, P = decltype(Pc)::value, slot = bslot(C);
    if constexpr (C < 18) {
      if constexpr (P < 4) dma16<0>(m0gu + slot + P * 4096, voffGU[P], rGU, C * 32 * F_ * 2);
      else { if (xgu == (bool)(C & 1)) dma16<0>(m0gux + slot, voffGU[4], rGU, C * 32 * F_ * 2); }
    } else if constexpr (C < 27) {
      if constexpr (P < 4) dma16<0>(m0w1 + slot + P * 8192, voffWd, rWd, (C - 18) * 128 + P * 64 * 1152);
      else { if (xgu == (bool)(C & 1)) {
          const unsigned e = (g & 1) + 2 * hf;
          dma16<0>(lds0 + (32 + e) * 1024 + slot, voffWd, rWd, (C - 18) * 128 + ((32 + e) - w) * 8 * 1152); } }
    }
  };
  const unsigned voffP = (rb * 16 + 4 * g + ((lane2 & 31) >> 3)) * 147456 + (4 * hd + h_) * 2048 + j * 128 + (lane2 & 7) * 16;
  {
    const rsrc_t rP = mkrsrc(ws + WS_P, 16 * ACT_B);
    unsigned aOwn = lds0 + (17 & 3) * A_SLOT + trb + cp * 8 + hd * 512 + hd * 16384, aPar = aOwn ^ 16384;
    PIN(aOwn); PIN(aPar);
    f32x16 acc[2];
    auto stage = [&](auto Nc) AI {
      constexpr int n = decltype(Nc)::value, i = n >> 4, ks = n & 15, C = 17 + i;
      if constexpr (ks == 0 && i > 0) {
        bump(aOwn, ((C & 3) - ((C - 1) & 3)) * A_SLOT); bump(aPar, ((C & 3) - ((C - 1) & 3)) * A_SLOT);
        wait_bar<(C < 24 ? 8 : 6) + (i >= 3 ? 4 : i == 2 ? 2 : 0)>();
      }
      const lptr b = lp(ks < 8 ? aOwn : aPar) + (2 * (ks & 7)) * 1024;
      st.F[n & 7] = trfrag(b, b + 1024);
      if constexpr (C < 23) { if constexpr ((ks & 3) == 1) issue(std::integral_constant<int, C + 3>{}, std::integral_constant<int, (ks >> 2)>{}); }
      else if constexpr (C == 23) {
        if constexpr (ks == 1) dma16<0>(m0w1 + SP_X, lane2 * 16 + w * 1024, mkrsrc(x, S_ * D_ * 2), (rb * 128 + j * 8) * 1152);
        if constexpr (ks == 5) {
          const unsigned pc = w == 0 ? 0 : (w < 6 ? w - 1 : 4);
          dma16<0>(lds0 + (w == 0 ? SP_X + 8192 : B_SPARE + pc * 1024), lane2 * 16, w == 0 ? mkrsrc(x, S_ * D_ * 2) : mkrsrc(ws + WS_TABB, 4608),
                   w == 0 ? (rb * 128 + j * 8) * 1152 + 8192 : pc * 1024);
        }
      } else { if constexpr (ks % 3 == 1) issueF(std::integral_constant<int, C - 24>{}, std::integral_constant<int, ks / 3>{}); }
    };
    sfor<0, 4>([&](auto N) AI { stage(N); SBAR(); });
    sfor<0, 144>([&](auto Mc) AI {
      constexpr int m = decltype(Mc)::value, i = m >> 4, ks = m & 15;
      if constexpr (m + 4 < 144) stage(std::integral_constant<int, m + 4>{});
      if constexpr (ks == 0) acc[i & 1] = MFMA(st.F[m & 7], Of[0], f32x16{});
      else acc[i & 1] = MFMA(st.F[m & 7], Of[ks], acc[i & 1]);
      if constexpr (i > 0 && (ks == 3 || ks == 5)) { constexpr int s = (ks - 3) >> 1;
        __builtin_amdgcn_raw_buffer_store_b128(pack8(acc[(i - 1) & 1], s), rP, voffP, (i - 1) * 16384 + s * 4096, 16); }
      SBAR();
    });
    __builtin_amdgcn_raw_buffer_store_b128(pack8(acc[0], 0), rP, voffP, 8 * 16384, 16);
    __builtin_amdgcn_raw_buffer_store_b128(pack8(acc[0], 1), rP, voffP, 8 * 16384 + 4096, 16);
  }
  STAMP(5);
  unsigned* cnt = (unsigned*)(ws + WS_CNT) + rb * 64;
  auto gather = [&](size_t wsoff, float (&y)[9], auto NXc, auto&& extra) AI {
    constexpr int NX = decltype(NXc)::value;
    const rsrc_t rP = mkrsrc(ws + wsoff, 16 * ACT_B);
    unsigned ln = lane; PIN(ln);
    const unsigned vo = (rb * 16 + j) * 147456 + w * 2048 + ln * 16;
    u32x4 pv[18];
    sfor<0, 9>([&](auto Tc) AI { constexpr int t = decltype(Tc)::value; gld16c<0>(pv[2 * t], vo, rP, t * 16384); gld16c<1024>(pv[2 * t + 1], vo, rP, t * 16384); });
    extra();
    const bool b3 = (ln >> 3) & 1;
    sfor<0, 9>([&](auto Tc) AI {
      constexpr int t = decltype(Tc)::value;
      asm volatile("s_waitcnt vmcnt(%2)" : "+v"(pv[2 * t]), "+v"(pv[2 * t + 1]) : "i"(16 - 2 * t + NX) : "memory");
      float sm[8];
#pragma unroll
      for (int e = 0; e < 4; ++e) { sm[2 * e] = bflo(pv[2 * t][e]) + bflo(pv[2 * t + 1][e]); sm[2 * e + 1] = bfhi(pv[2 * t][e]) + bfhi(pv[2 * t + 1][e]); }
      const float a0 = swap32_add(sm[0], sm[4]), a1 = swap32_add(sm[1], sm[5]), a2 = swap32_add(sm[2], sm[6]), a3 = swap32_add(sm[3], sm[7]);
      const float b0 = swap16_add(a0, a2), b1 = swap16_add(a1, a3);
      y[t] = (b3 ? b1 : b0) + ror8(b3 ? b0 : b1);
    });
  };
  auto rowsum = [&](float v, int slot) AI {
    v += ror8(v); v = swap16_add(v, v); v = swap32_add(v, v);
    if (lane < 8) *(LAS float*)(L + SP_RED + slot * 256 + lane * 32 + w * 4) = v;
    lbar();
    const f32x4 r0 = *(LAS f32x4*)(L + SP_RED + slot * 256 + (lane & 7) * 32), r1 = *(LAS f32x4*)(L + SP_RED + slot * 256 + (lane & 7) * 32 + 16);
    return ((r0[0] + r0[1]) + (r0[2] + r0[3])) + ((r1[0] + r1[1]) + (r1[2] + r1[3]));
  };
  float x2[9];
  {
    asm volatile("s_waitcnt vmcnt(0)\n\ts_barrier" ::: "memory");
    arrive_and_wait(cnt, tid);
    STAMP(6);
    float y[9];
    gather(WS_P, y, std::integral_constant<int, 4>{}, [&]() AI { sfor<0, 5>([&](auto P) AI { issueF(std::integral_constant<int, 2>{}, P); }); });
    const int e = lane >> 3, row8 = lane & 7, n0 = 16 * (w >> 1) + 4 * (w & 1) + 8 * (e >> 2) + (e & 3);
    const lptr tb = L + B_SPARE + n0 * 2, xr = L + SP_X + row8 * 1152 + n0 * 2;
    float s = 0.f;
#pragma unroll
    for (int t = 0; t < 9; ++t) { x2[t] = bflo(*(LAS u16*)(xr + t * 128)) + bflo(*(LAS u16*)(tb + t * 128)) * y[t]; s += x2[t]; }
    const float mean = rowsum(s, 0) * (1.f / D_);
    float q = 0.f;
#pragma unroll
    for (int t = 0; t < 9; ++t) { const float d = x2[t] - mean; q += d * d; }
    const float rstd = rsqrtf(rowsum(q, 1) * (1.f / D_) + LN_EPS);
    const lptr sc = L + bslot(3) + w * 1152;
#pragma unroll
    for (int t = 0; t < 9; ++t) {
      const float hv = (x2[t] - mean) * rstd * bflo(*(LAS u16*)(tb + 2 * 1152 + t * 128)) + bflo(*(LAS u16*)(tb + 1152 + t * 128));
      *(LAS u16*)(sc + t * 128 + row8 * 16 + e * 2) = (u16)cvtpk(hv, hv);
    }
    asm volatile("s_waitcnt lgkmcnt(0)" ::: "memory");
    const u32x4 c0 = *(LAS u32x4*)(sc + lane * 16), c1 = *(LAS u32x4*)(sc + 1024 + row8 * 16);
    const unsigned ho = (((rb * 4 + (j >> 2)) * NKS + (w >> 1)) * 64 + (w & 1) * 32 + (j & 3) * 8 + row8) * 16;
    __builtin_amdgcn_raw_buffer_store_b128(c0, rH2, ho + e * 4096, 0, 16);
    if (lane < 8) __builtin_amdgcn_raw_buffer_store_b128(c1, rH2, ho + 8 * 4096, 0, 16);
    STAMP(7);
    asm volatile("s_waitcnt vmcnt(0)\n\ts_barrier" ::: "memory");
    arrive_and_wait(cnt + 16 * 64, tid);
    STAMP(8);
  }
  bf16x8 F[8], Af[18];
  unsigned lane3 = lane; PIN(lane3);
  const int trb3 = (lane3 >> 5) * 256 + ((lane3 >> 2) & 3) * 64 + (lane3 & 3) * 8, blk3 = (lane3 >> 4) & 1;
  {
    const unsigned voffH = lane3 * 16 + rg * (NKS * 1024);
    u32x4 H[NKS];
    auto issueH = [&](auto Cc, auto Pc) AI {
      constexpr int C = decltype(Cc)::value, P = decltype(Pc)::value;
      if constexpr (C < 18) gld16c<1024 * P>(H[2 * C + P], voffH, rH2, C * 2048);
    };
    sfor<0, 3>([&](auto C) AI { sfor<0, 2>([&](auto P) AI { issueH(C, P); }); });
    unsigned aE = lds0 + bslot(0) + trb3 + blk3 * 18432 + hf * 2048 + ((hf ^ blk3) * 32);
    unsigned aO = lds0 + bslot(0) + trb3 + blk3 * 18432 + hf * 2560 + ((1 ^ hf ^ blk3) * 32);
    PIN(aE); PIN(aO);
    voffWd = (288 * j + 8 * w + ((lane3 >> 2) & 7)) * 1152 + (lane3 >> 5) * 64 + (lane3 & 3) * 16;
    f32x16 acc[9];
#pragma unroll
    for (int t = 0; t < 9; ++t) acc[t] = f32x16{};
    auto stage = [&](auto Nc) AI {
      constexpr int n = decltype(Nc)::value, ks = n / 9, T = n % 9, C = n / 18, r = n % 18, ksl = ks & 1;
      if constexpr (r == 0) {
        if constexpr (C > 0) { bump(aE, bslot(C) - bslot(C - 1)); bump(aO, bslot(C) - bslot(C - 1)); }
        constexpr int NV = C == 0 ? 4 : C == 1 ? 8 : 9 + (C + 1 < 18 ? 2 : 0) + (C + 2 < 18 ? 2 : 0);
        asm volatile("s_waitcnt vmcnt(%2) lgkmcnt(0)\n\ts_barrier" : "+v"(H[2 * C]), "+v"(H[2 * C + 1]) : "i"(NV) : "memory");
      }
      const lptr b = lp((T & 1) ? aO : aE) + ((2 * ksl) * 9 + (T >> 1)) * 512;
      F[n & 7] = trfrag(b, b + 9 * 512);
      if constexpr (C + 3 != 20) {
        if constexpr (r == 0 || r == 2) issueH(std::integral_constant<int, C + 3>{}, std::integral_constant<int, (r >> 1)>{});
        if constexpr (r >= 4 && r < 14 && (r & 1) == 0) issueF(std::integral_constant<int, C + 3>{}, std::integral_constant<int, ((r - 4) >> 1)>{});
      }
    };
    sfor<0, 4>([&](auto N) AI { stage(N); SBAR(); });
    STAMP(9);
    sfor<0, 324>([&](auto Mc) AI {
      constexpr int m = decltype(Mc)::value;
      if constexpr (m + 4 < 324) stage(std::integral_constant<int, m + 4>{});
      acc[m % 9] = MFMA(F[m & 7], asfrag(H[m / 9]), acc[m % 9]);
      SBAR();
    });
    STAMP(10);
    lbar();
    const lptr xw = L + bslot(16) + w * 9216 + lane3 * 16, xr = L + bslot(16) + (w ^ 4) * 9216 + lane3 * 16;
#pragma unroll
    for (int t = 0; t < 9; ++t) {
      float a[8];
#pragma unroll
      for (int e = 0; e < 8; ++e) {
        const float gv = acc[t][e];
        a[e] = gv * acc[t][8 + e] * __builtin_amdgcn_rcpf(1.f + __builtin_amdgcn_exp2f(gv * -1.4426950408889634f));
      }
      u32x4 pk = {cvtpk(a[0], a[1]), cvtpk(a[2], a[3]), cvtpk(a[4], a[5]), cvtpk(a[6], a[7])};
      Af[t] = asfrag(pk);
      *(LAS bf16x8*)(xw + t * 1024) = Af[t];
    }
    wait_bar<0>();
#pragma unroll
    for (int t = 0; t < 9; ++t) Af[9 + t] = ldfrag(xr + t * 1024);
    lbar();
    SBAR();
    sfor<0, 5>([&](auto P) AI { issueF(std::integral_constant<int, 20>{}, P); });
  }
  STAMP(11);
  {
    const rsrc_t rP = mkrsrc(ws + WS_P2, 16 * ACT_B);
    const unsigned voffP2 = (rb * 16 + 4 * g + ((lane3 & 31) >> 3)) * 147456 + (4 * hf + (lane3 >> 5)) * 2048 + j * 128 + (lane3 & 7) * 16;
    const unsigned aT = lds0 + bslot(18) + trb3 + blk3 * 32 + hf * 512;
    unsigned aOwn = aT + hf * 18432, aPar = aT + (1 - hf) * 18432;
    PIN(aOwn); PIN(aPar);
    f32x16 acc[2];
    auto stage = [&](auto Nc) AI {
      constexpr int n = decltype(Nc)::value, i = n / 18, ks = n % 18, C = 18 + i;
      if constexpr (ks == 0 && i > 0) {
        bump(aOwn, bslot(C) - bslot(C - 1)); bump(aPar, bslot(C) - bslot(C - 1));
        constexpr int NV = (C + 2 < 27 ? 9 : (C + 1 < 27 ? 4 : 0)) + (i >= 3 ? 4 : i == 2 ? 2 : 0);
        wait_bar<NV>();
      }
      const lptr b = lp(ks < 9 ? aOwn : aPar) + (2 * (ks % 9)) * 1024;
      F[n & 7] = trfrag(b, b + 1024);
      if constexpr (ks >= 1 && ks < 16 && (ks % 3) == 1) issueF(std::integral_constant<int, C + 3>{}, std::integral_constant<int, (ks / 3)>{});
    };
    sfor<0, 4>([&](auto N) AI { stage(N); SBAR(); });
    sfor<0, 162>([&](auto Mc) AI {
      constexpr int m = decltype(Mc)::value, i = m / 18, ks = m % 18;
      if constexpr (m + 4 < 162) stage(std::integral_constant<int, m + 4>{});
      if constexpr (ks == 0) acc[i & 1] = MFMA(F[m & 7], Af[0], f32x16{});
      else acc[i & 1] = MFMA(F[m & 7], Af[ks], acc[i & 1]);
      if constexpr (i > 0 && (ks == 3 || ks == 5)) { constexpr int s = (ks - 3) >> 1;
        __builtin_amdgcn_raw_buffer_store_b128(pack8(acc[(i - 1) & 1], s), rP, voffP2, (i - 1) * 16384 + s * 4096, 16); }
      SBAR();
    });
    __builtin_amdgcn_raw_buffer_store_b128(pack8(acc[0], 0), rP, voffP2, 8 * 16384, 16);
    __builtin_amdgcn_raw_buffer_store_b128(pack8(acc[0], 1), rP, voffP2, 8 * 16384 + 4096, 16);
  }
  STAMP(12);
  {
    asm volatile("s_waitcnt vmcnt(0)\n\ts_barrier" ::: "memory");
    arrive_and_wait(cnt + 32 * 64, tid);
    STAMP(13);
    float y[9];
    gather(WS_P2, y, std::integral_constant<int, 0>{}, [&]() AI {});
    unsigned ln = lane; PIN(ln);
    const int e = ln >> 3, row8 = ln & 7, n0 = 16 * (w >> 1) + 4 * (w & 1) + 8 * (e >> 2) + (e & 3);
    const lptr tb = L + B_SPARE + 3 * 1152 + n0 * 2, im = L + row8 * 1152 + n0 * 2;
#pragma unroll
    for (int t = 0; t < 9; ++t) {
      const float ov = x2[t] + bflo(*(LAS u16*)(tb + t * 128)) * y[t];
      *(LAS u16*)(im + t * 128) = (u16)cvtpk(ov, ov);
    }
    lbar();
    u16* op = out + (size_t)(rb * 128 + j * 8) * D_;
    const unsigned t2 = w * 64 + ln;
    *(u32x4*)(op + t2 * 8) = *(LAS u32x4*)(L + t2 * 16);
    if (w == 0) *(u32x4*)(op + 4096 + ln * 8) = *(LAS u32x4*)(L + 8192 + ln * 16);
  }
  STAMP(14);
}

extern "C" void kernel_launch(void* const* d_in, const int* in_sizes, int n_in, void* d_out, int out_size, void* d_ws, size_t ws_size,
                              hipStream_t stream) {
  (void)in_sizes; (void)n_in; (void)out_size; (void)ws_size;
  const u16* x = (const u16*)d_in[0]; const u16* kc = (const u16*)d_in[1]; const u16* vc = (const u16*)d_in[2];
  const u16* wq = (const u16*)d_in[3]; const u16* wo = (const u16*)d_in[4];
  const u16* wg = (const u16*)d_in[6]; const u16* wu = (const u16*)d_in[7]; const u16* wd = (const u16*)d_in[8];
  const u16* c = (const u16*)d_in[9]; const u16* wmod = (const u16*)d_in[10];
  char* ws = (char*)d_ws;
  (void)hipFuncSetAttribute((const void*)kmain, hipFuncAttributeMaxDynamicSharedMemorySize, A_LDS);
  k0_prep<<<108 + 256, 576, 0, stream>>>(x, c, wmod, (unsigned*)(ws + WS_CNT), (float*)(ws + WS_TABA), (u16*)(ws + WS_TABB), (u16*)(ws + WS_XH));
  kmain<<<256, 512, A_LDS, stream>>>(x, wq, kc, vc, wo, wg, wu, wd, ws, (u16*)d_out);
}
```

```cpp
#include <hip/hip_runtime.h>
#include <stdint.h>
#include <type_traits>

typedef short    bf16x8 __attribute__((ext_vector_type(8)));
typedef short    s16x4  __attribute__((ext_vector_type(4)));
typedef float    f32x16 __attribute__((ext_vector_type(16)));
typedef float    f32x4  __attribute__((ext_vector_type(4)));
typedef float    f32x2  __attribute__((ext_vector_type(2)));
typedef __bf16   bf16x2 __attribute__((ext_vector_type(2)));
typedef unsigned u32x4  __attribute__((ext_vector_type(4)));
typedef unsigned u32x2  __attribute__((ext_vector_type(2)));
typedef unsigned short u16;
#define LAS __attribute__((address_space(3)))
typedef LAS char* lptr;
typedef __amdgpu_buffer_rsrc_t rsrc_t;
#define DI __device__ __forceinline__
#define AI __attribute__((always_inline))
#define SBAR() __builtin_amdgcn_sched_barrier(0)
#define PIN(x) asm volatile("" : "+v"(x))
#define MFMA(a, b, c) __builtin_amdgcn_mfma_f32_32x32x16_bf16((a), (b), (c), 0, 0, 0)

constexpr int S_ = 2048, D_ = 576, HQ = 32, HKV = 16, SK = 2048, DH = 128, WIN = 512, F_ = 4608;
constexpr int NKS = D_ / 16;
constexpr int NRG = S_ / 32;
constexpr float LN_EPS = 1e-5f;
constexpr float QSCALE = 0.08838834764831845f * 1.4426950408889634f;

constexpr unsigned WS_FLAG = 0;
constexpr unsigned WS_EPOCH = 16384;
constexpr unsigned WS_GRAN = 20480;
constexpr size_t WS_XH   = 65536;
constexpr size_t ACT_B   = (size_t)S_ * D_ * 2;
constexpr size_t WS_H2   = WS_XH + ACT_B;
constexpr size_t WS_X2   = WS_H2 + ACT_B;
constexpr size_t WS_P    = WS_X2 + 2 * ACT_B;
constexpr size_t WS_P2   = WS_P;
constexpr size_t WS_DBG  = WS_P + 32 * ACT_B;

DI unsigned rfl(unsigned x) { return __builtin_amdgcn_readfirstlane(x); }
DI unsigned cvtpk(float lo, float hi) { f32x2 v = {lo, hi}; bf16x2 b = __builtin_convertvector(v, bf16x2); return __builtin_bit_cast(unsigned, b); }
DI float bflo(unsigned w) { return __uint_as_float(w << 16); }
DI float bfhi(unsigned w) { return __uint_as_float(w & 0xffff0000u); }
DI float bfr(float x) { return bflo(cvtpk(x, x)); }
DI rsrc_t mkrsrc(const void* p, unsigned bytes) { return __builtin_amdgcn_make_buffer_rsrc((void*)p, (short)0, (int)bytes, 0x00020000); }
template <int OFF> DI void dma16(unsigned m0v, unsigned voff, rsrc_t r, unsigned soff) {
  asm volatile("s_mov_b32 m0, %0\n\ts_nop 0\n\tbuffer_load_dwordx4 %1, %2, %3 offen offset:%4 lds"
               :: "s"(m0v), "v"(voff), "s"(r), "s"(soff), "i"(OFF) : "memory");
}
DI void touch(unsigned m0v, unsigned voff, rsrc_t r) {
  asm volatile("s_mov_b32 m0, %0\n\ts_nop 0\n\tbuffer_load_dword %1, %2, 0 offen lds" :: "s"(m0v), "v"(voff), "s"(r) : "memory");
}
template <int OFF> DI void gld16(u32x4& d, unsigned voff, rsrc_t r, unsigned soff) {
  asm volatile("buffer_load_dwordx4 %0, %1, %2, %3 offen offset:%4" : "=v"(d) : "v"(voff), "s"(r), "s"(soff), "i"(OFF) : "memory");
}
template <int OFF> DI void gld16c(u32x4& d, unsigned voff, rsrc_t r, unsigned soff) {
  asm volatile("buffer_load_dwordx4 %0, %1, %2, %3 offen offset:%4 sc1" : "=v"(d) : "v"(voff), "s"(r), "s"(soff), "i"(OFF) : "memory");
}
DI s16x4 trd(lptr p) { return __builtin_amdgcn_ds_read_tr16_b64_v4i16((LAS s16x4*)p); }
DI bf16x8 trfrag(lptr p0, lptr p1) { s16x4 lo = trd(p0), hi = trd(p1); return __builtin_shufflevector(lo, hi, 0, 1, 2, 3, 4, 5, 6, 7); }
DI bf16x8 ldfrag(lptr p) { return *(LAS bf16x8*)p; }
DI bf16x8 asfrag(u32x4 w) { return __builtin_bit_cast(bf16x8, w); }
DI float swap_max(float m) { auto rr = __builtin_amdgcn_permlane32_swap(__float_as_uint(m), __float_as_uint(m), false, false);
  return __builtin_fmaxf(__uint_as_float(rr[0]), __uint_as_float(rr[1])); }
DI float swap_add(float m) { auto rr = __builtin_amdgcn_permlane32_swap(__float_as_uint(m), __float_as_uint(m), false, false);
  return __uint_as_float(rr[0]) + __uint_as_float(rr[1]); }
DI float swap16_add(float a, float b) { auto rr = __builtin_amdgcn_permlane16_swap(__float_as_uint(a), __float_as_uint(b), false, false);
  return __uint_as_float(rr[0]) + __uint_as_float(rr[1]); }
DI float swap32_add(float a, float b) { auto rr = __builtin_amdgcn_permlane32_swap(__float_as_uint(a), __float_as_uint(b), false, false);
  return __uint_as_float(rr[0]) + __uint_as_float(rr[1]); }
DI float ror8(float a) { return __int_as_float(__builtin_amdgcn_update_dpp(0, __float_as_int(a), 0x128, 0xf, 0xf, false)); }
DI void lbar() { asm volatile("s_waitcnt lgkmcnt(0)\n\ts_barrier" ::: "memory"); }
DI void signal(rsrc_t r, unsigned soff, u32x2 tag, int tid) { if (tid == 0) __builtin_amdgcn_raw_buffer_store_b64(tag, r, 0, soff, 16); }
DI void gld8c(u32x2& v, unsigned voff, rsrc_t r, unsigned soff) { asm volatile("buffer_load_dwordx2 %0, %1, %2, %3 offen sc1" : "=v"(v) : "v"(voff), "s"(r), "s"(soff) : "memory"); }
DI bool all_lanes(bool c) { return __builtin_amdgcn_ballot_w64(c) == ~0ull; }
DI void poll_until(u32x2 v, unsigned voff, rsrc_t r, unsigned soff, u32x2 tag) {
  for (int sp = 0; !all_lanes(v[0] == tag[0] && v[1] == tag[1]) && sp < (1 << 20); ++sp) {
    __builtin_amdgcn_s_sleep(1);
    asm volatile("buffer_load_dwordx2 %0, %1, %2, %3 offen sc1\n\ts_waitcnt vmcnt(0)" : "=v"(v) : "v"(voff), "s"(r), "s"(soff) : "memory");
  }
}
DI u32x4 pack8(const f32x16& a, int s) {
  u32x4 w = {cvtpk(a[8 * s], a[8 * s + 1]), cvtpk(a[8 * s + 2], a[8 * s + 3]), cvtpk(a[8 * s + 4], a[8 * s + 5]), cvtpk(a[8 * s + 6], a[8 * s + 7])};
  return w;
}
DI lptr lp(unsigned a) { return (lptr)(uintptr_t)a; }
DI void bump(unsigned& a, int d) { a += d; PIN(a); }
template <int N> DI void wait_bar() { asm volatile("s_waitcnt vmcnt(%0) lgkmcnt(0)\n\ts_barrier" :: "i"(N) : "memory"); }
template <int I, int N, class F> DI void sfor(F&& f) { if constexpr (I < N) { f(std::integral_constant<int, I>{}); sfor<I + 1, N>(f); } }

#define STAMP(i)

constexpr int A_SLOT = 32768, A_SPARE = 4 * A_SLOT, A_LDS = 5 * A_SLOT;
constexpr float L_LIMIT = 1.2379400392853803e27f;
constexpr float M_MARGIN = 40.f;

#define WSTEP 128
#define WSTEP_N(b) ((b) / WSTEP)
struct AttnState {
  f32x16 O[4], S[2], negm; u32x4 Pk[2][2]; bf16x8 Q[8], F[8]; float l;
};

constexpr int B_SLOT = 36864, B_SPARE = 4 * B_SLOT, SP_X = B_SPARE + 5120, SP_RED = B_SPARE + 14336;
constexpr int bslot(int C) { return ((C + 2) & 3) * B_SLOT; }

__global__ __launch_bounds__(512, 2) void kmain(const u16* __restrict__ x, const u16* __restrict__ wq, const u16* __restrict__ kc,
                                                const u16* __restrict__ vc, const u16* __restrict__ wo, const u16* __restrict__ wg,
                                                const u16* __restrict__ wu, const u16* __restrict__ wd, const u16* __restrict__ cvec,
                                                const u16* __restrict__ wmod, char* __restrict__ ws, u16* __restrict__ out) {
  const u16* xh = (const u16*)(ws + WS_XH);
  extern __shared__ __attribute__((aligned(16))) char smem[];
  const lptr L = (lptr)smem;
  const unsigned lds0 = (unsigned)(uintptr_t)L;
  const int tid = threadIdx.x, lane = tid & 63;
  const unsigned w = rfl(tid >> 6), g = w & 3, hd = w >> 2;
  const unsigned j = 2 * (blockIdx.x & 7) + ((blockIdx.x >> 3) & 1), rb = blockIdx.x >> 4, rg = rb * 4 + g;
  const int r32 = lane & 31, h = lane >> 5, r8 = (lane >> 2) & 7, cp = lane & 3, blk = (lane >> 4) & 1, q4 = (lane >> 2) & 3;
  STAMP(0);
  const rsrc_t rWq = mkrsrc(wq, D_ * 4096 * 2), rK = mkrsrc(kc, HKV * SK * DH * 2), rV = mkrsrc(vc, HKV * SK * DH * 2),
               rWo = mkrsrc(wo, 4096 * D_ * 2), rX = mkrsrc(xh, S_ * D_ * 2);
  const unsigned voffWq = (16 * (w >> 1) + 8 * (r8 >> 2) + 4 * (w & 1) + (r8 & 3)) * 8192 + h * 64 + cp * 16 + 512 * j;
  const unsigned voffX = lane * 16 + rg * (NKS * 1024);
  const unsigned kvrow = 8 * (w >> 1) + r8;
  const unsigned voffV = kvrow * 256 + (2 * (w & 1) + h) * 64 + cp * 16;
  const unsigned voffK = kvrow * 256 + (2 * (w & 1) + h) * 64 + ((cp ^ ((kvrow >> 2) & 3)) * 16);
  const unsigned kvbase = (j * SK + (SK - WIN)) * 256;
  const unsigned voffWo = (256 * j + 32 * w + r8) * 1152 + h * 64 + cp * 16;
  const unsigned m0w4 = lds0 + w * 4096, m0w1 = lds0 + w * 1024;

  auto issue = [&](auto Cc, auto Pc) AI {
    constexpr int C = decltype(Cc)::value, P = decltype(Pc)::value, slot = (C & 3) * A_SLOT;
    if constexpr (C < 9) dma16<128 * P>(m0w4 + slot + P * 896, voffWq, rWq, C * 64 * 8192);
    else if constexpr (C < 17) {
      constexpr int i = C - 9;
      constexpr bool isK = (P < 2 && !(i == 7 && P == 1)) || (i == 0 && P == 2);
      constexpr int u = (i == 0 && P == 2) ? 0 : (i == 7 && P == 1) ? 15 : (P < 2 ? 2 * i + 1 + P : 2 * i - 3 + P);
      if constexpr (isK) dma16<0>(m0w1 + slot + P * 8192, voffK, rK, kvbase + u * 8192);
      else               dma16<0>(m0w1 + slot + P * 8192, voffV, rV, kvbase + u * 8192);
    } else if constexpr (C < 26) dma16<0>(m0w4 + slot + P * 1024, voffWo, rWo, (C - 17) * 128 + P * 8 * 1152);
  };
  u32x4 X[NKS];
  auto issueX = [&](auto Cc, auto Pc) AI {
    constexpr int C = decltype(Cc)::value, P = decltype(Pc)::value;
    if constexpr (C < 9) gld16c<1024 * P>(X[4 * C + P], voffX, rX, C * 4096);
  };
  const rsrc_t rC = mkrsrc(ws, 65536), rXr = mkrsrc(x, S_ * D_ * 2);
  const unsigned voffF = (lane & 15) * 8;
  u32x2 tag;
  {
    const int e = lane >> 3, row8 = lane & 7;
    u32x2 ev; u32x4 wm[3], xa, xb; unsigned cv[3];
    asm volatile("buffer_load_dwordx2 %0, off, %1, %2 sc1" : "=v"(ev) : "s"(rC), "s"(WS_EPOCH) : "memory");
    dma16<0>(m0w1 + A_SPARE + 4096, e * 1152 + row8 * 16 + w * 128, rXr, (rb * 128 + j * 8) * 1152);
    gld16<1024>(xb, row8 * 1152 + w * 16, rXr, (rb * 128 + j * 8) * 1152);
    const rsrc_t rM = mkrsrc(wmod, D_ * 3456 * 2), rCv = mkrsrc(cvec, D_ * 2);
    const unsigned pb = ((blockIdx.x & 7) + 8 * (blockIdx.x >> 5)) * 4 + ((blockIdx.x >> 3) & 3);
    const unsigned vm = (tid >> 1) * 6912 + (tid & 1) * 16 + pb * 32;
#define NC_DELAY 0
    if (NC_DELAY && pb >= 72) __builtin_amdgcn_s_sleep(NC_DELAY);
#pragma unroll
    for (int i = 0; i < 3; ++i) {
      gld16<0>(wm[i], vm, rM, i * 256 * 6912);
      asm volatile("buffer_load_ushort %0, %1, %2, %3 offen" : "=v"(cv[i]) : "v"(tid & ~1), "s"(rCv), "s"(i * 512) : "memory");
    }
#define EARLYC 0
    sfor<0, EARLYC>([&](auto C) AI { sfor<0, 4>([&](auto P) AI { issue(C, P); }); });
    asm volatile("s_waitcnt vmcnt(%8)" : "+v"(ev), "+v"(wm[0]), "+v"(wm[1]), "+v"(wm[2]), "+v"(cv[0]), "+v"(cv[1]), "+v"(cv[2]), "+v"(xb) : "i"(4 * EARLYC) : "memory");
    xa = *(LAS u32x4*)(L + A_SPARE + 4096 + w * 1024 + row8 * 128 + e * 16);
    STAMP(16);
    { const unsigned long long t = (((unsigned long long)rfl(ev[1]) << 32) | rfl(ev[0])) + 1ull; tag[0] = (unsigned)t; tag[1] = (unsigned)(t >> 32); }
    const unsigned gl = tag[0] << 16, gh = (tag[1] << 16) | (tag[0] >> 16);
    float a[8];
#pragma unroll
    for (int q = 0; q < 8; ++q) a[q] = 0.f;
#pragma unroll
    for (int i = 0; i < 3; ++i) {
      const float c0 = bflo(cv[i]), sc = bfr(c0 * __builtin_amdgcn_rcpf(1.f + __builtin_amdgcn_exp2f(c0 * -1.4426950408889634f)));
#pragma unroll
      for (int q = 0; q < 4; ++q) { a[2 * q] += sc * bflo(wm[i][q]); a[2 * q + 1] += sc * bfhi(wm[i][q]); }
    }
    const bool b3 = e & 1;
    const float a0 = swap32_add(a[0], a[4]), a1 = swap32_add(a[1], a[5]), a2 = swap32_add(a[2], a[6]), a3 = swap32_add(a[3], a[7]);
    const float b0 = swap16_add(a0, a2), b1 = swap16_add(a1, a3);
    float ms = (b3 ? b1 : b0) + ror8(b3 ? b0 : b1);
    ms += __shfl_xor(ms, 2); ms += __shfl_xor(ms, 4);
    const lptr red = L + A_SPARE;
    if ((lane & 6) == 0) *(LAS float*)(red + ((lane & 1) * 8 + e) * 32 + w * 4) = ms;
    float v[16];
#pragma unroll
    for (int q = 0; q < 4; ++q) { v[2 * q] = bflo(xa[q]); v[2 * q + 1] = bfhi(xa[q]); v[8 + 2 * q] = bflo(xb[q]); v[9 + 2 * q] = bfhi(xb[q]); }
    float s0 = 0.f, s1 = 0.f;
#pragma unroll
    for (int q = 0; q < 8; ++q) { s0 += v[q]; s1 += v[8 + q]; }
    s0 += e == 0 ? s1 : 0.f;
    s0 += ror8(s0); s0 = swap16_add(s0, s0); s0 = swap32_add(s0, s0);
    if (lane < 8) *(LAS float*)(red + 512 + lane * 32 + w * 4) = s0;
    lbar();
    auto sum8 = [&](lptr q) AI { const f32x4 r0 = *(LAS f32x4*)q, r1 = *(LAS f32x4*)(q + 16);
      return ((r0[0] + r0[1]) + (r0[2] + r0[3])) + ((r1[0] + r1[1]) + (r1[2] + r1[3])); };
    if (tid < 16 && pb < 216) {
      float m = bfr(sum8(red + tid * 32));
      const unsigned vec = pb / 36;
      if (vec == 1 || vec == 4) m = bfr(1.f + m);
      const u32x2 gr = {gl | (__float_as_uint(m) >> 16), gh};
      __builtin_amdgcn_raw_buffer_store_b64(gr, rC, tid * 8, WS_GRAN + pb * 128, 16);
    }
    STAMP(17);
    const float mean = sum8(red + 512 + row8 * 32) * (1.f / D_);
    s0 = 0.f; s1 = 0.f;
#pragma unroll
    for (int q = 0; q < 8; ++q) { v[q] -= mean; v[8 + q] -= mean; s0 += v[q] * v[q]; s1 += v[8 + q] * v[8 + q]; }
    s0 += e == 0 ? s1 : 0.f;
    s0 += ror8(s0); s0 = swap16_add(s0, s0); s0 = swap32_add(s0, s0);
    if (lane < 8) *(LAS float*)(red + 768 + lane * 32 + w * 4) = s0;
#define MIDC 2
    sfor<EARLYC, MIDC>([&](auto C) AI { sfor<0, 4>([&](auto P) AI { issue(C, P); }); });
    const unsigned i2 = tid < 128 ? tid + 1024 : tid;
    u32x2 g0, g1, g2;
    for (int sp = 0; sp < (1 << 20); ++sp) {
      asm volatile("buffer_load_dwordx2 %0, %3, %5, %6 offen sc1\n\tbuffer_load_dwordx2 %1, %3, %5, %7 offen sc1\n\tbuffer_load_dwordx2 %2, %4, %5, %6 offen sc1\n\ts_waitcnt vmcnt(0)"
                   : "=&v"(g0), "=&v"(g1), "=&v"(g2) : "v"(tid * 8), "v"(i2 * 8), "s"(rC), "s"(WS_GRAN), "s"(WS_GRAN + 4096) : "memory");
      if (all_lanes((((g0[0] ^ gl) | (g1[0] ^ gl) | (g2[0] ^ gl)) >> 16 | (g0[1] ^ gh) | (g1[1] ^ gh) | (g2[1] ^ gh)) == 0u)) break;
#define SWEEP_SLEEP 1
      __builtin_amdgcn_s_sleep(SWEEP_SLEEP);
    }
    const lptr tab = red + 1024;
    *(LAS u16*)(tab + tid * 2) = (u16)g0[0]; *(LAS u16*)(tab + 1024 + tid * 2) = (u16)g1[0]; *(LAS u16*)(tab + i2 * 2) = (u16)g2[0];
    lbar();
    STAMP(15);
    const float rs = rsqrtf(sum8(red + 768 + row8 * 32) * (1.f / D_) + LN_EPS);
    const u32x4 sa = *(LAS u32x4*)(tab + w * 128 + e * 16), ca = *(LAS u32x4*)(tab + 1152 + w * 128 + e * 16);
    const u32x4 sb = *(LAS u32x4*)(tab + 1024 + w * 16), cb = *(LAS u32x4*)(tab + 1152 + 1024 + w * 16);
    u32x4 ha, hb;
#pragma unroll
    for (int q = 0; q < 4; ++q) {
      ha[q] = cvtpk((v[2 * q] * rs * bflo(ca[q]) + bflo(sa[q])) * QSCALE, (v[2 * q + 1] * rs * bfhi(ca[q]) + bfhi(sa[q])) * QSCALE);
      hb[q] = cvtpk((v[8 + 2 * q] * rs * bflo(cb[q]) + bflo(sb[q])) * QSCALE, (v[9 + 2 * q] * rs * bfhi(cb[q]) + bfhi(sb[q])) * QSCALE);
    }
    const unsigned ho = ((rb * 4 + (j >> 2)) * NKS * 64 + (j & 3) * 8 + row8) * 16;
    __builtin_amdgcn_raw_buffer_store_b128(ha, rX, ho + (8 * w + e) * 512, 0, 16);
    if (lane < 8) __builtin_amdgcn_raw_buffer_store_b128(hb, rX, ho + (64 + w) * 512, 0, 16);
    asm volatile("s_waitcnt vmcnt(0)\n\ts_barrier" ::: "memory");
    STAMP(18);
    signal(rC, 7 * 2048 + rb * 128 + j * 8, tag, tid);
    sfor<(MIDC > EARLYC ? MIDC : EARLYC), 4>([&](auto C) AI { sfor<0, 4>([&](auto P) AI { issue(C, P); }); });
    poll_until(u32x2{~tag[0], 0u}, voffF, rC, 7 * 2048 + rb * 128, tag);
    STAMP(19);
  }
  sfor<0, 3>([&](auto C) AI { sfor<0, 4>([&](auto P) AI { issueX(C, P); }); });

  const int trb = h * 256 + q4 * 64 + blk * 32;
  unsigned aWq = lds0 + trb + (((cp & 1) << 1) | (cp >> 1)) * 8 + hd * 2048;
  PIN(aWq);
  AttnState st;
  {
    f32x16 acc[4];
#pragma unroll
    for (int t = 0; t < 4; ++t) acc[t] = f32x16{};
    auto stage = [&](auto Nc) AI {
      constexpr int n = decltype(Nc)::value, ks = n >> 2, T = n & 3, C = n >> 4, ksl = ks & 3;
      if constexpr ((n & 15) == 0) {
        if constexpr (C > 0) bump(aWq, ((C & 3) - ((C - 1) & 3)) * A_SLOT);
        constexpr int NV = C < 2 ? 8 : C == 2 ? 12 : (C + 1 < 9 ? 8 : 4) + (C + 2 < 9 ? 8 : 4);
        asm volatile("s_waitcnt vmcnt(%4) lgkmcnt(0)\n\ts_barrier" : "+v"(X[4 * C]), "+v"(X[4 * C + 1]), "+v"(X[4 * C + 2]), "+v"(X[4 * C + 3]) : "i"(NV) : "memory");
      }
      st.F[n & 7] = trfrag(lp(aWq) + ((2 * ksl) * 8 + T) * 512, lp(aWq) + ((2 * ksl + 1) * 8 + T) * 512);
      if constexpr (n & 1) {
        if constexpr (n & 2) { if constexpr (C > 0) issue(std::integral_constant<int, C + 3>{}, std::integral_constant<int, ((n & 15) >> 2)>{}); }
        else issueX(std::integral_constant<int, C + 3>{}, std::integral_constant<int, ((n & 15) >> 2)>{});
      }
    };
    sfor<0, 4>([&](auto N) AI { stage(N); SBAR(); });
    STAMP(1);
    sfor<0, 144>([&](auto Mc) AI {
      constexpr int m = decltype(Mc)::value;
      if constexpr (m + 4 < 144) stage(std::integral_constant<int, m + 4>{});
      acc[m & 3] = MFMA(st.F[m & 7], asfrag(X[m >> 2]), acc[m & 3]);
      SBAR();
    });
#pragma unroll
    for (int t = 0; t < 4; ++t) { st.Q[2 * t] = asfrag(pack8(acc[t], 0)); st.Q[2 * t + 1] = asfrag(pack8(acc[t], 1)); }
  }
  STAMP(2);
  unsigned aK0 = lds0 + (9 & 3) * A_SLOT + 2048 * (r32 >> 3) + 64 * (r32 & 7) + 16 * ((0 + h) ^ ((r32 >> 2) & 3));
  unsigned aK1 = aK0 ^ 32, aV = lds0 + (9 & 3) * A_SLOT + trb + cp * 8;
  PIN(aK0); PIN(aK1); PIN(aV);
#pragma unroll
  for (int t = 0; t < 4; ++t) st.O[t] = f32x16{};
  auto kfrag = [&](int off, auto Ic) AI { constexpr int i = decltype(Ic)::value; return ldfrag(lp((i & 1) ? aK1 : aK0) + off + 512 * (i >> 1)); };
  auto vfrag = [&](int off, auto Ic) AI { constexpr int i = decltype(Ic)::value, ks = i >> 2, T = i & 3;
    return trfrag(lp(aV) + off + ((2 * ks) * 4 + T) * 512, lp(aV) + off + ((2 * ks + 1) * 4 + T) * 512); };
  auto step = [&](auto Par, auto HQK, auto HPV, auto NXT, auto NVc, int u, int ko, int vo, int kn, int dsl, auto&& dmaf) AI {
    constexpr int par = decltype(Par)::value, nxt = decltype(NXT)::value, NV = decltype(NVc)::value;
    constexpr bool hqk = decltype(HQK)::value, hpv = decltype(HPV)::value;
    float l = st.l, eprev = 0.f;
    sfor<0, 16>([&](auto Mc) AI {
      constexpr int m = decltype(Mc)::value, n = m + 4;
      if constexpr (n < 8) { if constexpr (hqk) st.F[n & 7] = kfrag(ko, std::integral_constant<int, n>{}); }
      else if constexpr (n < 16) { if constexpr (hpv) st.F[n & 7] = vfrag(vo, std::integral_constant<int, n - 8>{}); }
      else {
        if constexpr (n == 16 && NV >= 0) { bump(aK0, dsl); bump(aK1, dsl); bump(aV, dsl); wait_bar<NV>(); }
        if constexpr (nxt == 1) st.F[n & 7] = kfrag(kn, std::integral_constant<int, n - 16>{});
      }
      dmaf(Mc);
      if constexpr (m < 8) { if constexpr (hqk) st.S[par ^ 1] = MFMA(st.F[m & 7], st.Q[m], m == 0 ? st.negm : st.S[par ^ 1]); }
      else { if constexpr (hpv) st.O[m & 3] = MFMA(st.F[m & 7], asfrag(st.Pk[par ^ 1][(m - 8) >> 2]), st.O[m & 3]); }
      const float e = __builtin_amdgcn_exp2f(st.S[par][m]);
      l += e; PIN(l);
      if constexpr (m & 1) { st.Pk[par][m >> 3][(m >> 1) & 3] = cvtpk(eprev, e); PIN(st.Pk[par][m >> 3]); }
      else { eprev = e; PIN(eprev); }
      SBAR();
    });
    st.l = l;
  };
  using I0 = std::integral_constant<int, 0>; using I1 = std::integral_constant<int, 1>; using IM = std::integral_constant<int, -1>;
  using BT = std::true_type; using BF = std::false_type;
  {
    wait_bar<8>();
    sfor<0, 4>([&](auto P) AI { issue(std::integral_constant<int, 12>{}, P); });
    f32x16 s = f32x16{};
    sfor<0, 8>([&](auto I) AI { s = MFMA(kfrag(2 * 8192, I), st.Q[decltype(I)::value], s); });
    sfor<0, 4>([&](auto I) AI { st.F[decltype(I)::value] = kfrag(0, I); });
    float mt = s[0];
#pragma unroll
    for (int r = 1; r < 16; ++r) mt = __builtin_fmaxf(mt, s[r]);
    mt = swap_max(mt) + M_MARGIN;
#pragma unroll
    for (int r = 0; r < 16; ++r) { st.S[0][r] = s[r] - mt; st.negm[r] = -mt; }
    PIN(st.negm);
    st.l = 0.f;
    SBAR();
  }
  auto nodma = [&](auto) AI {};
  sfor<0, 8>([&](auto Ic) AI {
    constexpr int i = decltype(Ic)::value, C = 9 + i, dsl = (((C + 1) & 3) - (C & 3)) * A_SLOT;
    auto dmaf = [&](auto Mc) AI { constexpr int m = decltype(Mc)::value;
      if constexpr (i > 0 && (m & 3) == 1) issue(std::integral_constant<int, C + 3>{}, std::integral_constant<int, (m >> 2)>{}); };
    if constexpr (i == 0) step(I0{}, BT{}, BF{}, I1{}, IM{}, 0, 0, 0, 8192, 0, dmaf);
    else if constexpr (i < 7) step(I0{}, BT{}, BT{}, I1{}, IM{}, 2 * i, 0, 2 * 8192, 8192, 0, dmaf);
    else step(I0{}, BT{}, BT{}, I0{}, IM{}, 2 * i, 0, 2 * 8192, 0, 0, dmaf);
    auto warm = [&](auto Mc) AI { constexpr int m = decltype(Mc)::value;
      if constexpr (m == 13) {
        unsigned t = tid; PIN(t);
        if constexpr (i == 0) touch(lds0 + A_SPARE, (256 * j + 16 * rb) * 1152 + (t < WSTEP_N(18432) ? t : 0) * WSTEP, rWo);
        if constexpr (i == 1 || (i == 2 && WSTEP == 64)) { const unsigned u = (t & 255) + (i - 1) * 256, nl = 640 / WSTEP, rr = u / nl, cc = u - rr * nl;
          touch(lds0 + A_SPARE, (36 * rb + (rr < 36 ? rr : 0)) * (F_ * 2) + ((576 * j) & ~127u) + cc * WSTEP, mkrsrc(hd ? wu : wg, D_ * F_ * 2)); }
        if constexpr (i == 3) touch(lds0 + A_SPARE, (288 * j + 18 * rb) * 1152 + (t < WSTEP_N(20736) ? t : 0) * WSTEP, mkrsrc(wd, F_ * D_ * 2));
      }
    };
    if constexpr (i < 7) step(I1{}, BT{}, BT{}, I1{}, std::integral_constant<int, 8>{}, 2 * i + 1, 8192, 3 * 8192, 0, dsl, warm);
    else step(I1{}, BF{}, BT{}, I0{}, IM{}, 15, 0, 3 * 8192, 0, 0, nodma);
  });
  bf16x8 Of[16];
  {
    constexpr int s16 = (16 & 3) * A_SLOT;
    sfor<0, 8>([&](auto Ic) AI { constexpr int i = decltype(Ic)::value; st.O[i & 3] = MFMA(vfrag(8192, Ic), asfrag(st.Pk[1][i >> 2]), st.O[i & 3]); });
    STAMP(3);
    asm volatile("s_waitcnt lgkmcnt(0)\n\ts_barrier" ::: "memory");
    if (__builtin_expect(__builtin_amdgcn_ballot_w64(!(st.l <= L_LIMIT)) != 0, 0)) {
      const u16* kg = kc + ((size_t)j * SK + (SK - WIN) + r32) * DH + 8 * h;
      const u16* vg = vc + ((size_t)j * SK + (SK - WIN) + 4 * h) * DH + r32;
      float mt = -__builtin_inff();
#pragma unroll 1
      for (int u = 0; u < 16; ++u) {
        f32x16 s = f32x16{};
#pragma unroll
        for (int i = 0; i < 8; ++i) s = MFMA(*(const bf16x8*)(kg + (size_t)u * 32 * DH + 16 * i), st.Q[i], s);
#pragma unroll
        for (int r = 0; r < 16; ++r) mt = __builtin_fmaxf(mt, s[r]);
      }
      mt = swap_max(mt);
      float l = 0.f;
#pragma unroll
      for (int t = 0; t < 4; ++t) st.O[t] = f32x16{};
#pragma unroll 1
      for (int u = 0; u < 16; ++u) {
        f32x16 s = f32x16{};
#pragma unroll
        for (int i = 0; i < 8; ++i) s = MFMA(*(const bf16x8*)(kg + (size_t)u * 32 * DH + 16 * i), st.Q[i], s);
#pragma unroll
        for (int r = 0; r < 16; ++r) { s[r] = __builtin_amdgcn_exp2f(s[r] - mt); l += s[r]; }
#pragma unroll
        for (int ks = 0; ks < 2; ++ks) {
          const bf16x8 pf = asfrag(pack8(s, ks));
#pragma unroll
          for (int t = 0; t < 4; ++t) {
            bf16x8 vf;
#pragma unroll
            for (int e = 0; e < 8; ++e) vf[e] = (short)vg[(size_t)(u * 32 + 16 * ks + 8 * (e >> 2) + (e & 3)) * DH + 32 * t];
            st.O[t] = MFMA(vf, pf, st.O[t]);
          }
        }
      }
      st.l = l;
    }
    const float inv = __builtin_amdgcn_rcpf(swap_add(st.l));
#pragma unroll
    for (int t = 0; t < 4; ++t) {
#pragma unroll
      for (int r = 0; r < 16; ++r) st.O[t][r] *= inv;
      Of[2 * t] = asfrag(pack8(st.O[t], 0)); Of[2 * t + 1] = asfrag(pack8(st.O[t], 1));
    }
    const lptr xw = L + (hd ? s16 : A_SPARE) + g * 8192 + lane * 16, xr = L + (hd ? A_SPARE : s16) + g * 8192 + lane * 16;
#pragma unroll
    for (int i = 0; i < 8; ++i) *(LAS bf16x8*)(xw + i * 1024) = Of[i];
    wait_bar<8>();
#pragma unroll
    for (int i = 0; i < 8; ++i) Of[8 + i] = ldfrag(xr + i * 1024);
    asm volatile("s_waitcnt lgkmcnt(0)\n\ts_barrier" ::: "memory");
    SBAR();
  }
  STAMP(4);
  const unsigned hf = hd;
  const rsrc_t rGU = mkrsrc(hf ? wu : wg, D_ * F_ * 2), rWd = mkrsrc(wd, F_ * D_ * 2), rH2 = mkrsrc(ws + WS_H2, S_ * D_ * 2);
  unsigned lane2 = lane; PIN(lane2);
  const int h_ = lane2 >> 5, r8_ = (lane2 >> 2) & 7, cp_ = lane2 & 3;
  unsigned voffGU[5];
#pragma unroll
  for (int p = 0; p < 5; ++p) {
    const int piece = p < 4 ? (int)g + 4 * p : 16 + (int)(g & 1), stl = 2 * piece + h_, R = stl / 9, sub = stl - 9 * R;
    voffGU[p] = (8 * R + r8_) * (F_ * 2) + 576 * j + sub * 64 + ((cp_ ^ (hf ? 2 : 0)) * 16);
  }
  const unsigned m0gu = lds0 + hf * 18432 + g * 1024, m0gux = lds0 + hf * 18432 + (16 + (g & 1)) * 1024;
  const bool xgu = (g >> 1) != 0;
  unsigned voffWd = 0;
  auto issueF = [&](auto Cc, auto Pc) AI {
    constexpr int C = decltype(Cc)::value, P = decltype(Pc)::value, slot = bslot(C);
    if constexpr (C < 18) {
      if constexpr (P < 4) dma16<0>(m0gu + slot + P * 4096, voffGU[P], rGU, C * 32 * F_ * 2);
      else { if (xgu == (bool)(C & 1)) dma16<0>(m0gux + slot, voffGU[4], rGU, C * 32 * F_ * 2); }
    } else if constexpr (C < 27) {
      if constexpr (P < 4) dma16<0>(m0w1 + slot + P * 8192, voffWd, rWd, (C - 18) * 128 + P * 64 * 1152);
      else { if (xgu == (bool)(C & 1)) {
          const unsigned e = (g & 1) + 2 * hf;
          dma16<0>(lds0 + (32 + e) * 1024 + slot, voffWd, rWd, (C - 18) * 128 + ((32 + e) - w) * 8 * 1152); } }
    }
  };
  const rsrc_t rP = mkrsrc(ws + WS_P, 16 * ACT_B);
  auto project = [&](auto NKc, auto CBc, const bf16x8* Bf, unsigned aOwn, unsigned aPar, auto&& dslot, auto&& nv, auto&& dma, auto NXc, auto&& extra, float (&y)[9]) AI {
    constexpr int NK = decltype(NKc)::value, HK = NK / 2, CB = decltype(CBc)::value, NX = decltype(NXc)::value;
    unsigned ln = lane; PIN(ln);
    const unsigned voffP = (rb * 16 + 4 * g + ((ln & 31) >> 3)) * 147456 + (4 * hd + (ln >> 5)) * 2048 + j * 128 + (ln & 7) * 16;
    const unsigned vo = (rb * 16 + j) * 147456 + w * 2048 + ln * 16;
    const unsigned co = rb * 128 + CB * 2048;
    PIN(aOwn); PIN(aPar);
    bf16x8 F[8]; f32x16 acc[2]; u32x4 pv[18]; u32x2 pl;
    const unsigned vf = (ln & 15) * 8;
    auto ldp = [&](auto Tc) AI { constexpr int t = decltype(Tc)::value; gld16c<(t & 1) * 1024>(pv[t], vo, rP, (t >> 1) * 16384); };
    auto red = [&](auto Tc) AI {
      constexpr int t = decltype(Tc)::value;
      const bool b3 = (ln >> 3) & 1;
      float sm[8];
#pragma unroll
      for (int e = 0; e < 4; ++e) { sm[2 * e] = bflo(pv[2 * t][e]) + bflo(pv[2 * t + 1][e]); sm[2 * e + 1] = bfhi(pv[2 * t][e]) + bfhi(pv[2 * t + 1][e]); }
      const float a0 = swap32_add(sm[0], sm[4]), a1 = swap32_add(sm[1], sm[5]), a2 = swap32_add(sm[2], sm[6]), a3 = swap32_add(sm[3], sm[7]);
      const float b0 = swap16_add(a0, a2), b1 = swap16_add(a1, a3);
      y[t] = (b3 ? b1 : b0) + ror8(b3 ? b0 : b1);
    };
    auto stage = [&](auto Nc) AI {
      constexpr int n = decltype(Nc)::value, i = n / NK, ks = n % NK;
      if constexpr (ks == 0 && i > 0) {
        bump(aOwn, dslot(i)); bump(aPar, dslot(i));
        constexpr int NV = decltype(nv(std::integral_constant<int, i>{}))::value;
        if constexpr (i == 8) {
          asm volatile("s_waitcnt vmcnt(%1)" : "+v"(pl) : "i"(NV) : "memory");
          poll_until(pl, vf, rC, co, tag);
          lbar();
        } else wait_bar<NV>();
        if constexpr (i == 5) signal(rC, co + j * 8, tag, tid);
        if constexpr (i == 7) gld8c(pl, vf, rC, co);
        if constexpr (i == 8) signal(rC, co + 2048 + j * 8, tag, tid);
      }
      const lptr b = lp(ks < HK ? aOwn : aPar) + (2 * (ks % HK)) * 1024;
      F[n & 7] = trfrag(b, b + 1024);
      if constexpr (i == 8 && ks < 6) ldp(std::integral_constant<int, ks>{});
      dma(std::integral_constant<int, i>{}, std::integral_constant<int, ks>{});
    };
    sfor<0, 4>([&](auto N) AI { stage(N); SBAR(); });
    sfor<0, 9 * NK>([&](auto Mc) AI {
      constexpr int m = decltype(Mc)::value, i = m / NK, ks = m % NK;
      if constexpr (m + 4 < 9 * NK) stage(std::integral_constant<int, m + 4>{});
      if constexpr (ks == 0) acc[i & 1] = MFMA(F[m & 7], Bf[0], f32x16{});
      else acc[i & 1] = MFMA(F[m & 7], Bf[ks], acc[i & 1]);
      if constexpr (i > 0 && (ks == 3 || ks == 5)) { constexpr int s = (ks - 3) >> 1;
        __builtin_amdgcn_raw_buffer_store_b128(pack8(acc[(i - 1) & 1], s), rP, voffP, (i - 1) * 16384 + s * 4096, 16); }
      SBAR();
    });
    __builtin_amdgcn_raw_buffer_store_b128(pack8(acc[0], 0), rP, voffP, 8 * 16384, 16);
    __builtin_amdgcn_raw_buffer_store_b128(pack8(acc[0], 1), rP, voffP, 8 * 16384 + 4096, 16);
    STAMP(CB == 0 ? 5 : 12);
    gld8c(pl, vf, rC, co + 2048);
    asm volatile("s_waitcnt vmcnt(0) lgkmcnt(0)\n\ts_barrier" : "+v"(pl), "+v"(pv[0]), "+v"(pv[1]), "+v"(pv[2]), "+v"(pv[3]), "+v"(pv[4]), "+v"(pv[5]) :: "memory");
    signal(rC, co + 4096 + j * 8, tag, tid);
    poll_until(pl, vf, rC, co + 2048, tag);
    sfor<6, 12>(ldp);
    sfor<0, 3>(red);
    gld8c(pl, vf, rC, co + 4096);
    asm volatile("s_waitcnt vmcnt(0)" : "+v"(pl), "+v"(pv[6]), "+v"(pv[7]), "+v"(pv[8]), "+v"(pv[9]), "+v"(pv[10]), "+v"(pv[11]) :: "memory");
    poll_until(pl, vf, rC, co + 4096, tag);
    STAMP(CB == 0 ? 6 : 13);
    sfor<12, 18>(ldp);
    extra();
    sfor<3, 6>(red);
    sfor<6, 9>([&](auto Tc) AI {
      constexpr int t = decltype(Tc)::value;
      asm volatile("s_waitcnt vmcnt(%2)" : "+v"(pv[2 * t]), "+v"(pv[2 * t + 1]) : "i"(16 - 2 * t + NX) : "memory");
      red(Tc);
    });
  };
  auto rowsum = [&](float v, int slot) AI {
    v += ror8(v); v = swap16_add(v, v); v = swap32_add(v, v);
    if (lane < 8) *(LAS float*)(L + SP_RED + slot * 256 + lane * 32 + w * 4) = v;
    lbar();
    const f32x4 r0 = *(LAS f32x4*)(L + SP_RED + slot * 256 + (lane & 7) * 32), r1 = *(LAS f32x4*)(L + SP_RED + slot * 256 + (lane & 7) * 32 + 16);
    return ((r0[0] + r0[1]) + (r0[2] + r0[3])) + ((r1[0] + r1[1]) + (r1[2] + r1[3]));
  };
  float x2[9];
  {
    float y[9]; u32x2 gb[5];
    const unsigned aOwn = lds0 + (17 & 3) * A_SLOT + trb + cp * 8 + hd * 512 + hd * 16384;
    project(std::integral_constant<int, 16>{}, I0{}, Of, aOwn, aOwn ^ 16384,
            [](int i) AI { return (((17 + i) & 3) - ((16 + i) & 3)) * A_SLOT; },
            [](auto Ic) AI { constexpr int i = decltype(Ic)::value;
              return std::integral_constant<int, i == 5 ? 7 : i == 8 ? 6 : i == 7 ? 14 : 8 + (i >= 3 ? 4 : i == 2 ? 2 : 0)>{}; },
            [&](auto Ic, auto Kc) AI {
              constexpr int C = 17 + decltype(Ic)::value, ks = decltype(Kc)::value;
              if constexpr (C == 25 && ks == 8) {
                unsigned t = tid; PIN(t);
                const unsigned gl = tag[0] << 16, gh = (tag[1] << 16) | (tag[0] >> 16), i4 = (t & 255) + 2048;
                asm volatile("" : "+v"(gb[0]), "+v"(gb[1]), "+v"(gb[2]), "+v"(gb[3]), "+v"(gb[4]));
                for (int sp = 0; sp < (1 << 20); ++sp) {
                  unsigned bad = 0;
#pragma unroll
                  for (int q = 0; q < 5; ++q) bad |= ((gb[q][0] ^ gl) >> 16) | (gb[q][1] ^ gh);
                  if (__builtin_expect(all_lanes(bad == 0u), 1)) break;
#pragma unroll
                  for (int q = 0; q < 5; ++q) gld8c(gb[q], (q < 4 ? t + 512 * q : i4) * 8, rC, WS_GRAN + 1152 * 8);
                  asm volatile("s_waitcnt vmcnt(0)" : "+v"(gb[0]), "+v"(gb[1]), "+v"(gb[2]), "+v"(gb[3]), "+v"(gb[4]) :: "memory");
                }
#pragma unroll
                for (int q = 0; q < 5; ++q) *(LAS u16*)(L + B_SPARE + (q < 4 ? t + 512 * q : i4) * 2) = (u16)gb[q][0];
              }
              if constexpr (C < 23) { if constexpr ((ks & 3) == 1) issue(std::integral_constant<int, C + 3>{}, std::integral_constant<int, (ks >> 2)>{}); }
              else if constexpr (C == 23) {
                if constexpr (ks == 1) dma16<0>(m0w1 + SP_X, lane2 * 16 + w * 1024, rXr, (rb * 128 + j * 8) * 1152);
                if constexpr (ks >= 3 && ks <= 11 && (ks & 1)) { constexpr int q = (ks - 3) >> 1; const unsigned t = w * 64 + lane2;
                  gld8c(gb[q], (q < 4 ? t + 512 * q : (t & 255) + 2048) * 8, rC, WS_GRAN + 1152 * 8); }
                if constexpr (ks == 13) { if (w == 0) dma16<0>(lds0 + SP_X + 8192, lane2 * 16, rXr, (rb * 128 + j * 8) * 1152 + 8192); }
              } else { if constexpr (ks % 3 == 1) issueF(std::integral_constant<int, C - 24>{}, std::integral_constant<int, ks / 3>{}); }
            },
            std::integral_constant<int, 4>{}, [&]() AI { sfor<0, 5>([&](auto P) AI { issueF(std::integral_constant<int, 2>{}, P); }); }, y);
    const int e = lane >> 3, row8 = lane & 7, n0 = 16 * (w >> 1) + 4 * (w & 1) + 8 * (e >> 2) + (e & 3);
    const lptr tb = L + B_SPARE + n0 * 2, xr = L + SP_X + row8 * 1152 + n0 * 2;
    float s = 0.f;
#pragma unroll
    for (int t = 0; t < 9; ++t) { x2[t] = bflo(*(LAS u16*)(xr + t * 128)) + bflo(*(LAS u16*)(tb + t * 128)) * y[t]; s += x2[t]; }
    const float mean = rowsum(s, 0) * (1.f / D_);
    float q = 0.f;
#pragma unroll
    for (int t = 0; t < 9; ++t) { const float d = x2[t] - mean; q += d * d; }
    const float rstd = rsqrtf(rowsum(q, 1) * (1.f / D_) + LN_EPS);
    const lptr sc = L + bslot(3) + w * 1152;
#pragma unroll
    for (int t = 0; t < 9; ++t) {
      const float hv = (x2[t] - mean) * rstd * bflo(*(LAS u16*)(tb + 2 * 1152 + t * 128)) + bflo(*(LAS u16*)(tb + 1152 + t * 128));
      *(LAS u16*)(sc + t * 128 + row8 * 16 + e * 2) = (u16)cvtpk(hv, hv);
    }
    asm volatile("s_waitcnt lgkmcnt(0)" ::: "memory");
    const u32x4 c0 = *(LAS u32x4*)(sc + lane * 16), c1 = *(LAS u32x4*)(sc + 1024 + row8 * 16);
    const unsigned ho = (((rb * 4 + (j >> 2)) * NKS + (w >> 1)) * 64 + (w & 1) * 32 + (j & 3) * 8 + row8) * 16;
    __builtin_amdgcn_raw_buffer_store_b128(c0, rH2, ho + e * 4096, 0, 16);
    if (lane < 8) __builtin_amdgcn_raw_buffer_store_b128(c1, rH2, ho + 8 * 4096, 0, 16);
    STAMP(7);
    asm volatile("s_waitcnt vmcnt(0)\n\ts_barrier" ::: "memory");
    signal(rC, 3 * 2048 + rb * 128 + j * 8, tag, tid);
    poll_until(u32x2{~tag[0], 0u}, (lane & 15) * 8, rC, 3 * 2048 + rb * 128, tag);
    STAMP(8);
  }
  bf16x8 F[8], Af[18];
  unsigned lane3 = lane; PIN(lane3);
  const int trb3 = (lane3 >> 5) * 256 + ((lane3 >> 2) & 3) * 64 + (lane3 & 3) * 8, blk3 = (lane3 >> 4) & 1;
  {
    const unsigned voffH = lane3 * 16 + rg * (NKS * 1024);
    u32x4 H[NKS];
    auto issueH = [&](auto Cc, auto Pc) AI {
      constexpr int C = decltype(Cc)::value, P = decltype(Pc)::value;
      if constexpr (C < 18) gld16c<1024 * P>(H[2 * C + P], voffH, rH2, C * 2048);
    };
    sfor<0, 3>([&](auto C) AI { sfor<0, 2>([&](auto P) AI { issueH(C, P); }); });
    unsigned aE = lds0 + bslot(0) + trb3 + blk3 * 18432 + hf * 2048 + ((hf ^ blk3) * 32);
    unsigned aO = lds0 + bslot(0) + trb3 + blk3 * 18432 + hf * 2560 + ((1 ^ hf ^ blk3) * 32);
    PIN(aE); PIN(aO);
    voffWd = (288 * j + 8 * w + ((lane3 >> 2) & 7)) * 1152 + (lane3 >> 5) * 64 + (lane3 & 3) * 16;
    f32x16 acc[9];
#pragma unroll
    for (int t = 0; t < 9; ++t) acc[t] = f32x16{};
    auto stage = [&](auto Nc) AI {
      constexpr int n = decltype(Nc)::value, ks = n / 9, T = n % 9, C = n / 18, r = n % 18, ksl = ks & 1;
      if constexpr (r == 0) {
        if constexpr (C > 0) { bump(aE, bslot(C) - bslot(C - 1)); bump(aO, bslot(C) - bslot(C - 1)); }
        constexpr int NV = C == 0 ? 4 : C == 1 ? 8 : 9 + (C + 1 < 18 ? 2 : 0) + (C + 2 < 18 ? 2 : 0);
        asm volatile("s_waitcnt vmcnt(%2) lgkmcnt(0)\n\ts_barrier" : "+v"(H[2 * C]), "+v"(H[2 * C + 1]) : "i"(NV) : "memory");
      }
      const lptr b = lp((T & 1) ? aO : aE) + ((2 * ksl) * 9 + (T >> 1)) * 512;
      F[n & 7] = trfrag(b, b + 9 * 512);
      if constexpr (C + 3 != 20) {
        if constexpr (r == 0 || r == 2) issueH(std::integral_constant<int, C + 3>{}, std::integral_constant<int, (r >> 1)>{});
        if constexpr (r >= 4 && r < 14 && (r & 1) == 0) issueF(std::integral_constant<int, C + 3>{}, std::integral_constant<int, ((r - 4) >> 1)>{});
      }
    };
    sfor<0, 4>([&](auto N) AI { stage(N); SBAR(); });
    STAMP(9);
    sfor<0, 324>([&](auto Mc) AI {
      constexpr int m = decltype(Mc)::value;
      if constexpr (m + 4 < 324) stage(std::integral_constant<int, m + 4>{});
      acc[m % 9] = MFMA(F[m & 7], asfrag(H[m / 9]), acc[m % 9]);
      SBAR();
    });
    STAMP(10);
    lbar();
    const lptr xw = L + bslot(16) + w * 9216 + lane3 * 16, xr = L + bslot(16) + (w ^ 4) * 9216 + lane3 * 16;
#pragma unroll
    for (int t = 0; t < 9; ++t) {
      float a[8];
#pragma unroll
      for (int e = 0; e < 8; ++e) {
        const float gv = acc[t][e];
        a[e] = gv * acc[t][8 + e] * __builtin_amdgcn_rcpf(1.f + __builtin_amdgcn_exp2f(gv * -1.4426950408889634f));
      }
      u32x4 pk = {cvtpk(a[0], a[1]), cvtpk(a[2], a[3]), cvtpk(a[4], a[5]), cvtpk(a[6], a[7])};
      Af[t] = asfrag(pk);
      *(LAS bf16x8*)(xw + t * 1024) = Af[t];
    }
    wait_bar<0>();
#pragma unroll
    for (int t = 0; t < 9; ++t) Af[9 + t] = ldfrag(xr + t * 1024);
    lbar();
    SBAR();
    sfor<0, 5>([&](auto P) AI { issueF(std::integral_constant<int, 20>{}, P); });
  }
  STAMP(11);
  {
    float y[9];
    const unsigned aT = lds0 + bslot(18) + trb3 + blk3 * 32 + hf * 512;
    project(std::integral_constant<int, 18>{}, std::integral_constant<int, 4>{}, Af, aT + hf * 18432, aT + (1 - hf) * 18432,
            [](int i) AI { return bslot(18 + i) - bslot(17 + i); },
            [](auto Ic) AI { constexpr int i = decltype(Ic)::value;
              return std::integral_constant<int, i == 5 ? 8 : i == 8 ? 2 : (i < 7 ? 9 : 4) + (i >= 3 ? 4 : i == 2 ? 2 : 0)>{}; },
            [&](auto Ic, auto Kc) AI {
              constexpr int C = 18 + decltype(Ic)::value, ks = decltype(Kc)::value;
              if constexpr (ks >= 1 && ks < 16 && (ks % 3) == 1) issueF(std::integral_constant<int, C + 3>{}, std::integral_constant<int, (ks / 3)>{});
            },
            I0{}, [&]() AI {}, y);
    unsigned ln = lane; PIN(ln);
    const int e = ln >> 3, row8 = ln & 7, n0 = 16 * (w >> 1) + 4 * (w & 1) + 8 * (e >> 2) + (e & 3);
    const lptr tb = L + B_SPARE + 3 * 1152 + n0 * 2, im = L + row8 * 1152 + n0 * 2;
#pragma unroll
    for (int t = 0; t < 9; ++t) {
      const float ov = x2[t] + bflo(*(LAS u16*)(tb + t * 128)) * y[t];
      *(LAS u16*)(im + t * 128) = (u16)cvtpk(ov, ov);
    }
    lbar();
    u16* op = out + (size_t)(rb * 128 + j * 8) * D_;
    const unsigned t2 = w * 64 + ln;
    *(u32x4*)(op + t2 * 8) = *(LAS u32x4*)(L + t2 * 16);
    if (w == 0) *(u32x4*)(op + 4096 + ln * 8) = *(LAS u32x4*)(L + 8192 + ln * 16);
  }
  if (blockIdx.x == 0 && w == 1) {
#pragma unroll 1
    for (int q = 0; q < 4; ++q) poll_until(u32x2{~tag[0], 0u}, lane * 8, rC, 7 * 2048 + q * 512, tag);
    if (lane == 0) __builtin_amdgcn_raw_buffer_store_b64(tag, rC, 0, WS_EPOCH, 16);
  }
  STAMP(14);
}

extern "C" void kernel_launch(void* const* d_in, const int* in_sizes, int n_in, void* d_out, int out_size, void* d_ws, size_t ws_size,
                              hipStream_t stream) {
  (void)in_sizes; (void)n_in; (void)out_size; (void)ws_size;
  const u16* x = (const u16*)d_in[0]; const u16* kc = (const u16*)d_in[1]; const u16* vc = (const u16*)d_in[2];
  const u16* wq = (const u16*)d_in[3]; const u16* wo = (const u16*)d_in[4];
  const u16* wg = (const u16*)d_in[6]; const u16* wu = (const u16*)d_in[7]; const u16* wd = (const u16*)d_in[8];
  const u16* c = (const u16*)d_in[9]; const u16* wmod = (const u16*)d_in[10];
  char* ws = (char*)d_ws;
  (void)hipFuncSetAttribute((const void*)kmain, hipFuncAttributeMaxDynamicSharedMemorySize, A_LDS);
  kmain<<<256, 512, A_LDS, stream>>>(x, wq, kc, vc, wo, wg, wu, wd, c, wmod, ws, (u16*)d_out);
}
```

```cpp
#include <hip/hip_runtime.h>
#include <stdint.h>
#include <type_traits>

typedef short    bf16x8 __attribute__((ext_vector_type(8)));
typedef short    s16x4  __attribute__((ext_vector_type(4)));
typedef float    f32x16 __attribute__((ext_vector_type(16)));
typedef float    f32x4  __attribute__((ext_vector_type(4)));
typedef float    f32x2  __attribute__((ext_vector_type(2)));
typedef __bf16   bf16x2 __attribute__((ext_vector_type(2)));
typedef unsigned u32x4  __attribute__((ext_vector_type(4)));
typedef unsigned u32x2  __attribute__((ext_vector_type(2)));
typedef unsigned short u16;
#define LAS __attribute__((address_space(3)))
typedef LAS char* lptr;
typedef __amdgpu_buffer_rsrc_t rsrc_t;
#define DI __device__ __forceinline__
#define AI __attribute__((always_inline))
#define SBAR() __builtin_amdgcn_sched_barrier(0)
#define PIN(x) asm volatile("" : "+v"(x))
#define MFMA(a, b, c) __builtin_amdgcn_mfma_f32_32x32x16_bf16((a), (b), (c), 0, 0, 0)

constexpr int S_ = 2048, D_ = 576, HQ = 32, HKV = 16, SK = 2048, DH = 128, WIN = 512, F_ = 4608;
constexpr int NKS = D_ / 16;
constexpr int NRG = S_ / 32;
constexpr float LN_EPS = 1e-5f;
constexpr float QSCALE = 0.08838834764831845f * 1.4426950408889634f;

constexpr unsigned WS_FLAG = 0;
constexpr unsigned WS_EPOCH = 16384;
constexpr unsigned WS_GRAN = 20480;
constexpr unsigned WS_TABB = 49152;
constexpr size_t WS_XH   = 65536;
constexpr size_t ACT_B   = (size_t)S_ * D_ * 2;
constexpr size_t WS_H2   = WS_XH + ACT_B;
constexpr size_t WS_X2   = WS_H2 + ACT_B;
constexpr size_t WS_P    = WS_X2 + 2 * ACT_B;
constexpr size_t WS_P2   = WS_P;
constexpr size_t WS_DBG  = WS_P + 32 * ACT_B;

DI unsigned rfl(unsigned x) { return __builtin_amdgcn_readfirstlane(x); }
DI unsigned cvtpk(float lo, float hi) { f32x2 v = {lo, hi}; bf16x2 b = __builtin_convertvector(v, bf16x2); return __builtin_bit_cast(unsigned, b); }
DI float bflo(unsigned w) { return __uint_as_float(w << 16); }
DI float bfhi(unsigned w) { return __uint_as_float(w & 0xffff0000u); }
DI float bfr(float x) { return bflo(cvtpk(x, x)); }
DI rsrc_t mkrsrc(const void* p, unsigned bytes) { return __builtin_amdgcn_make_buffer_rsrc((void*)p, (short)0, (int)bytes, 0x00020000); }
template <int OFF> DI void dma16(unsigned m0v, unsigned voff, rsrc_t r, unsigned soff) {
  asm volatile("s_mov_b32 m0, %0\n\ts_nop 0\n\tbuffer_load_dwordx4 %1, %2, %3 offen offset:%4 lds"
               :: "s"(m0v), "v"(voff), "s"(r), "s"(soff), "i"(OFF) : "memory");
}
DI void touch(unsigned m0v, unsigned voff, rsrc_t r) {
  asm volatile("s_mov_b32 m0, %0\n\ts_nop 0\n\tbuffer_load_dword %1, %2, 0 offen lds" :: "s"(m0v), "v"(voff), "s"(r) : "memory");
}
template <int OFF> DI void gld16(u32x4& d, unsigned voff, rsrc_t r, unsigned soff) {
  asm volatile("buffer_load_dwordx4 %0, %1, %2, %3 offen offset:%4" : "=v"(d) : "v"(voff), "s"(r), "s"(soff), "i"(OFF) : "memory");
}
template <int OFF> DI void gld16c(u32x4& d, unsigned voff, rsrc_t r, unsigned soff) {
  asm volatile("buffer_load_dwordx4 %0, %1, %2, %3 offen offset:%4 sc1" : "=v"(d) : "v"(voff), "s"(r), "s"(soff), "i"(OFF) : "memory");
}
DI s16x4 trd(lptr p) { return __builtin_amdgcn_ds_read_tr16_b64_v4i16((LAS s16x4*)p); }
DI bf16x8 trfrag(lptr p0, lptr p1) { s16x4 lo = trd(p0), hi = trd(p1); return __builtin_shufflevector(lo, hi, 0, 1, 2, 3, 4, 5, 6, 7); }
DI bf16x8 ldfrag(lptr p) { return *(LAS bf16x8*)p; }
DI bf16x8 asfrag(u32x4 w) { return __builtin_bit_cast(bf16x8, w); }
DI float swap_max(float m) { auto rr = __builtin_amdgcn_permlane32_swap(__float_as_uint(m), __float_as_uint(m), false, false);
  return __builtin_fmaxf(__uint_as_float(rr[0]), __uint_as_float(rr[1])); }
DI float swap_add(float m) { auto rr = __builtin_amdgcn_permlane32_swap(__float_as_uint(m), __float_as_uint(m), false, false);
  return __uint_as_float(rr[0]) + __uint_as_float(rr[1]); }
DI float swap16_add(float a, float b) { auto rr = __builtin_amdgcn_permlane16_swap(__float_as_uint(a), __float_as_uint(b), false, false);
  return __uint_as_float(rr[0]) + __uint_as_float(rr[1]); }
DI float swap32_add(float a, float b) { auto rr = __builtin_amdgcn_permlane32_swap(__float_as_uint(a), __float_as_uint(b), false, false);
  return __uint_as_float(rr[0]) + __uint_as_float(rr[1]); }
DI float ror8(float a) { return __int_as_float(__builtin_amdgcn_update_dpp(0, __float_as_int(a), 0x128, 0xf, 0xf, false)); }
DI void lbar() { asm volatile("s_waitcnt lgkmcnt(0)\n\ts_barrier" ::: "memory"); }
DI void signal(rsrc_t r, unsigned soff, u32x2 tag, unsigned prog, int tid) { if (tid == 0) __builtin_amdgcn_raw_buffer_store_b64(u32x2{tag[0] | prog, tag[1]}, r, 0, soff, 16); }
DI void gld8c(u32x2& v, unsigned voff, rsrc_t r, unsigned soff) { asm volatile("buffer_load_dwordx2 %0, %1, %2, %3 offen sc1" : "=v"(v) : "v"(voff), "s"(r), "s"(soff) : "memory"); }
DI bool all_lanes(bool c) { return __builtin_amdgcn_ballot_w64(c) == ~0ull; }
DI bool flag_ok(u32x2 v, u32x2 tag, unsigned need) { return v[1] == tag[1] && v[0] - (tag[0] + need) <= 15u - need; }
DI void poll_until(u32x2 v, unsigned voff, rsrc_t r, unsigned soff, u32x2 tag, unsigned need, unsigned own) {
  for (int sp = 0; !all_lanes(flag_ok(v, tag, need) || voff == own) && sp < (1 << 20); ++sp) {
    __builtin_amdgcn_s_sleep(2);
    asm volatile("buffer_load_dwordx2 %0, %1, %2, %3 offen sc1\n\ts_waitcnt vmcnt(0)" : "=v"(v) : "v"(voff), "s"(r), "s"(soff) : "memory");
  }
}
DI u32x4 pack8(const f32x16& a, int s) {
  u32x4 w = {cvtpk(a[8 * s], a[8 * s + 1]), cvtpk(a[8 * s + 2], a[8 * s + 3]), cvtpk(a[8 * s + 4], a[8 * s + 5]), cvtpk(a[8 * s + 6], a[8 * s + 7])};
  return w;
}
DI lptr lp(unsigned a) { return (lptr)(uintptr_t)a; }
DI void bump(unsigned& a, int d) { a += d; PIN(a); }
template <int N> DI void wait_bar() { asm volatile("s_waitcnt vmcnt(%0) lgkmcnt(0)\n\ts_barrier" :: "i"(N) : "memory"); }
template <int I, int N, class F> DI void sfor(F&& f) { if constexpr (I < N) { f(std::integral_constant<int, I>{}); sfor<I + 1, N>(f); } }

#define STAMP(i)

constexpr int A_SLOT = 32768, A_SPARE = 4 * A_SLOT, A_LDS = 5 * A_SLOT, A_JUNK = A_SPARE + 16384;
constexpr float L_LIMIT = 1.2379400392853803e27f;
constexpr float M_MARGIN = 40.f;

#define XNA 3
struct AttnState {
  f32x16 O[4], S[2], negm; u32x4 Pk[2][2]; bf16x8 Q[8], F[8]; float l;
};

constexpr int B_SLOT = 36864, B_SPARE = 4 * B_SLOT, SP_X = B_SPARE + 5120, SP_RED = B_SPARE + 14336;
constexpr int bslot(int C) { return ((C + 2) & 3) * B_SLOT; }

__global__ __launch_bounds__(512, 2) void kmain(const u16* __restrict__ x, const u16* __restrict__ wq, const u16* __restrict__ kc,
                                                const u16* __restrict__ vc, const u16* __restrict__ wo, const u16* __restrict__ wg,
                                                const u16* __restrict__ wu, const u16* __restrict__ wd, const u16* __restrict__ cvec,
                                                const u16* __restrict__ wmod, char* __restrict__ ws, u16* __restrict__ out) {
  const u16* xh = (const u16*)(ws + WS_XH);
  extern __shared__ __attribute__((aligned(16))) char smem[];
  const lptr L = (lptr)smem;
  const unsigned lds0 = (unsigned)(uintptr_t)L;
  const int tid = threadIdx.x, lane = tid & 63;
  const unsigned w = rfl(tid >> 6), g = w & 3, hd = w >> 2;
  const unsigned j = 2 * (blockIdx.x & 7) + ((blockIdx.x >> 3) & 1), rb = blockIdx.x >> 4, rg = rb * 4 + g;
  const int r32 = lane & 31, h = lane >> 5, r8 = (lane >> 2) & 7, cp = lane & 3, blk = (lane >> 4) & 1, q4 = (lane >> 2) & 3;
  STAMP(0);
  const rsrc_t rWq = mkrsrc(wq, D_ * 4096 * 2), rK = mkrsrc(kc, HKV * SK * DH * 2), rV = mkrsrc(vc, HKV * SK * DH * 2),
               rWo = mkrsrc(wo, 4096 * D_ * 2), rX = mkrsrc(xh, S_ * D_ * 2);
  const unsigned voffWq = (16 * (w >> 1) + 8 * (r8 >> 2) + 4 * (w & 1) + (r8 & 3)) * 8192 + h * 64 + cp * 16 + 512 * j;
  const unsigned voffX = lane * 16 + rg * (NKS * 1024);
  const unsigned kvrow = 8 * (w >> 1) + r8;
  const unsigned voffV = kvrow * 256 + (2 * (w & 1) + h) * 64 + cp * 16;
  const unsigned voffK = kvrow * 256 + (2 * (w & 1) + h) * 64 + ((cp ^ ((kvrow >> 2) & 3)) * 16);
  const unsigned kvbase = (j * SK + (SK - WIN)) * 256;
  const unsigned voffWo = (256 * j + 32 * w + r8) * 1152 + h * 64 + cp * 16;
  const unsigned m0w4 = lds0 + w * 4096, m0w1 = lds0 + w * 1024;

  auto issue = [&](auto Cc, auto Pc) AI {
    constexpr int C = decltype(Cc)::value, P = decltype(Pc)::value, slot = (C & 3) * A_SLOT;
    if constexpr (C < 9) dma16<128 * P>(m0w4 + slot + P * 896, voffWq, rWq, C * 64 * 8192);
    else if constexpr (C < 17) {
      constexpr int i = C - 9;
      constexpr bool isK = (P < 2 && !(i == 7 && P == 1)) || (i == 0 && P == 2);
      constexpr int u = (i == 0 && P == 2) ? 0 : (i == 7 && P == 1) ? 15 : (P < 2 ? 2 * i + 1 + P : 2 * i - 3 + P);
      if constexpr (isK) dma16<0>(m0w1 + slot + P * 8192, voffK, rK, kvbase + u * 8192);
      else               dma16<0>(m0w1 + slot + P * 8192, voffV, rV, kvbase + u * 8192);
    } else if constexpr (C < 26) dma16<0>(m0w4 + slot + P * 1024, voffWo, rWo, (C - 17) * 128 + P * 8 * 1152);
  };
  u32x4 X[NKS];
  auto issueX = [&](auto Cc, auto Pc) AI {
    constexpr int C = decltype(Cc)::value, P = decltype(Pc)::value;
    if constexpr (C < 9) gld16c<1024 * P>(X[4 * C + P], voffX, rX, C * 4096);
  };
  const rsrc_t rC = mkrsrc(ws, 65536), rXr = mkrsrc(x, S_ * D_ * 2);
  const unsigned voffF = (lane & 15) * 8;
  unsigned long long pc0; unsigned clen;
  asm volatile("s_getpc_b64 %0\n.Lkm_begin:\n\ts_mov_b32 %1, .Lkm_end-.Lkm_begin" : "=s"(pc0), "=s"(clen));
  const rsrc_t rCode = mkrsrc((const void*)pc0, clen);
  if (w == 7) touch(lds0 + A_JUNK, ((blockIdx.x >> 3) * 32 + (lane & 31)) * 128, rCode);
  u32x2 tag;
  {
    const int e = lane >> 3, row8 = lane & 7;
    u32x2 ev; u32x4 wm[3], xa, xb; unsigned cv[3];
    asm volatile("buffer_load_dwordx2 %0, off, %1, %2 sc1" : "=v"(ev) : "s"(rC), "s"(WS_EPOCH) : "memory");
    dma16<0>(m0w1 + A_SPARE + 4096, e * 1152 + row8 * 16 + w * 128, rXr, (rb * 128 + j * 8) * 1152);
    gld16<1024>(xb, row8 * 1152 + w * 16, rXr, (rb * 128 + j * 8) * 1152);
    const rsrc_t rM = mkrsrc(wmod, D_ * 3456 * 2), rCv = mkrsrc(cvec, D_ * 2);
    const unsigned pb = ((blockIdx.x & 7) + 8 * (blockIdx.x >> 5)) * 4 + ((blockIdx.x >> 3) & 3);
    const unsigned vm = (tid >> 1) * 6912 + (tid & 1) * 16 + pb * 32;
#pragma unroll
    for (int i = 0; i < 3; ++i) {
      gld16<0>(wm[i], vm, rM, i * 256 * 6912);
      asm volatile("buffer_load_ushort %0, %1, %2, %3 offen" : "=v"(cv[i]) : "v"(tid & ~1), "s"(rCv), "s"(i * 512) : "memory");
    }
    asm volatile("s_waitcnt vmcnt(0)" ::: "memory");
    if (w >= 6) {
      const unsigned u = tid - 384;
      touch(lds0 + A_JUNK, (36 * rb + (u >> 2)) * 8192 + 512 * j + (u & 3) * 128, rWq);
      if (w == 6) touch(lds0 + A_JUNK, (36 * rb + 32 + ((lane & 15) >> 2)) * 8192 + 512 * j + (u & 3) * 128, rWq);
    }
    asm volatile("" : "+v"(ev), "+v"(wm[0]), "+v"(wm[1]), "+v"(wm[2]), "+v"(cv[0]), "+v"(cv[1]), "+v"(cv[2]), "+v"(xb) :: "memory");
    xa = *(LAS u32x4*)(L + A_SPARE + 4096 + w * 1024 + row8 * 128 + e * 16);
    STAMP(16);
    { const unsigned long long t = (((unsigned long long)rfl(ev[1]) << 32) | rfl(ev[0])) + 16ull; tag[0] = (unsigned)t & ~15u; tag[1] = (unsigned)(t >> 32); }
    const unsigned gl = tag[0] << 12, gh = (tag[1] << 12) | (tag[0] >> 20);
    float a[8];
#pragma unroll
    for (int q = 0; q < 8; ++q) a[q] = 0.f;
#pragma unroll
    for (int i = 0; i < 3; ++i) {
      const float c0 = bflo(cv[i]), sc = bfr(c0 * __builtin_amdgcn_rcpf(1.f + __builtin_amdgcn_exp2f(c0 * -1.4426950408889634f)));
#pragma unroll
      for (int q = 0; q < 4; ++q) { a[2 * q] += sc * bflo(wm[i][q]); a[2 * q + 1] += sc * bfhi(wm[i][q]); }
    }
    const bool b3 = e & 1;
    const float a0 = swap32_add(a[0], a[4]), a1 = swap32_add(a[1], a[5]), a2 = swap32_add(a[2], a[6]), a3 = swap32_add(a[3], a[7]);
    const float b0 = swap16_add(a0, a2), b1 = swap16_add(a1, a3);
    float ms = (b3 ? b1 : b0) + ror8(b3 ? b0 : b1);
    ms += __shfl_xor(ms, 2); ms += __shfl_xor(ms, 4);
    const lptr red = L + A_SPARE;
    if ((lane & 6) == 0) *(LAS float*)(red + ((lane & 1) * 8 + e) * 32 + w * 4) = ms;
    float v[16];
#pragma unroll
    for (int q = 0; q < 4; ++q) { v[2 * q] = bflo(xa[q]); v[2 * q + 1] = bfhi(xa[q]); v[8 + 2 * q] = bflo(xb[q]); v[9 + 2 * q] = bfhi(xb[q]); }
    float s0 = 0.f, s1 = 0.f;
#pragma unroll
    for (int q = 0; q < 8; ++q) { s0 += v[q]; s1 += v[8 + q]; }
    s0 += e == 0 ? s1 : 0.f;
    s0 += ror8(s0); s0 = swap16_add(s0, s0); s0 = swap32_add(s0, s0);
    if (lane < 8) *(LAS float*)(red + 512 + lane * 32 + w * 4) = s0;
    lbar();
    auto sum8 = [&](lptr q) AI { const f32x4 r0 = *(LAS f32x4*)q, r1 = *(LAS f32x4*)(q + 16);
      return ((r0[0] + r0[1]) + (r0[2] + r0[3])) + ((r1[0] + r1[1]) + (r1[2] + r1[3])); };
    if (tid < 16 && pb < 216) {
      float m = bfr(sum8(red + tid * 32));
      const unsigned vec = pb / 36;
      if (vec == 1 || vec == 4) m = bfr(1.f + m);
      if (vec >= 2) {
        __builtin_amdgcn_raw_buffer_store_b16((u16)(__float_as_uint(m) >> 16), rC, tid * 2, WS_TABB + (pb - 72) * 32, 16);
        asm volatile("s_waitcnt vmcnt(0)" ::: "memory");
      }
      const u32x2 gr = {gl | (__float_as_uint(m) >> 16), gh};
      __builtin_amdgcn_raw_buffer_store_b64(gr, rC, tid * 8, WS_GRAN + pb * 128, 16);
    }
    STAMP(17);
    const float mean = sum8(red + 512 + row8 * 32) * (1.f / D_);
    s0 = 0.f; s1 = 0.f;
#pragma unroll
    for (int q = 0; q < 8; ++q) { v[q] -= mean; v[8 + q] -= mean; s0 += v[q] * v[q]; s1 += v[8 + q] * v[8 + q]; }
    s0 += e == 0 ? s1 : 0.f;
    s0 += ror8(s0); s0 = swap16_add(s0, s0); s0 = swap32_add(s0, s0);
    if (lane < 8) *(LAS float*)(red + 768 + lane * 32 + w * 4) = s0;
#define MIDC 2
    sfor<0, MIDC>([&](auto C) AI { sfor<0, 4>([&](auto P) AI { issue(C, P); }); });
    u32x2 g0, g1, g2;
    const lptr tab = red + 1024;
    if (w < 6) {
      for (int sp = 0; sp < (1 << 20); ++sp) {
        asm volatile("buffer_load_dwordx2 %0, %3, %4, %5 offen sc1\n\tbuffer_load_dwordx2 %1, %3, %4, %5 offen offset:3072 sc1\n\tbuffer_load_dwordx2 %2, %3, %4, %6 offen sc1\n\ts_waitcnt vmcnt(0)"
                     : "=&v"(g0), "=&v"(g1), "=&v"(g2) : "v"(tid * 8), "s"(rC), "s"(WS_GRAN), "s"(WS_GRAN + 6144) : "memory");
        if (all_lanes((((g0[0] ^ gl) | (g1[0] ^ gl) | (g2[0] ^ gl)) >> 16 | (g0[1] ^ gh) | (g1[1] ^ gh) | (g2[1] ^ gh)) == 0u)) break;
        __builtin_amdgcn_s_sleep(1);
      }
      *(LAS u16*)(tab + tid * 2) = (u16)g0[0]; *(LAS u16*)(tab + 768 + tid * 2) = (u16)g1[0]; *(LAS u16*)(tab + 1536 + tid * 2) = (u16)g2[0];
    }
    lbar();
    STAMP(15);
    const float rs = rsqrtf(sum8(red + 768 + row8 * 32) * (1.f / D_) + LN_EPS);
    const u32x4 sa = *(LAS u32x4*)(tab + w * 128 + e * 16), ca = *(LAS u32x4*)(tab + 1152 + w * 128 + e * 16);
    const u32x4 sb = *(LAS u32x4*)(tab + 1024 + w * 16), cb = *(LAS u32x4*)(tab + 1152 + 1024 + w * 16);
    u32x4 ha, hb;
#pragma unroll
    for (int q = 0; q < 4; ++q) {
      ha[q] = cvtpk((v[2 * q] * rs * bflo(ca[q]) + bflo(sa[q])) * QSCALE, (v[2 * q + 1] * rs * bfhi(ca[q]) + bfhi(sa[q])) * QSCALE);
      hb[q] = cvtpk((v[8 + 2 * q] * rs * bflo(cb[q]) + bflo(sb[q])) * QSCALE, (v[9 + 2 * q] * rs * bfhi(cb[q]) + bfhi(sb[q])) * QSCALE);
    }
    const unsigned ho = ((rb * 4 + (j >> 2)) * NKS * 64 + (j & 3) * 8 + row8) * 16;
    __builtin_amdgcn_raw_buffer_store_b128(ha, rX, ho + (8 * w + e) * 512, 0, 16);
    if (lane < 8) __builtin_amdgcn_raw_buffer_store_b128(hb, rX, ho + (64 + w) * 512, 0, 16);
    asm volatile("s_waitcnt vmcnt(0)\n\ts_barrier" ::: "memory");
    STAMP(18);
    signal(rC, 7 * 2048 + rb * 128 + j * 8, tag, 1, tid);
    sfor<MIDC, 4>([&](auto C) AI { sfor<0, 4>([&](auto P) AI { issue(C, P); }); });
    const unsigned vg = (tid < 144 ? tid : 0) * 128;
    for (int sp = 0; sp < (1 << 20); ++sp) {
      asm volatile("buffer_load_dwordx2 %0, %2, %4, %5 offen sc1\n\tbuffer_load_dwordx2 %1, %3, %4, %6 offen sc1\n\ts_waitcnt vmcnt(0)"
                   : "=&v"(g0), "=&v"(g1) : "v"(voffF), "v"(vg), "s"(rC), "s"(7 * 2048 + rb * 128), "s"(WS_GRAN + 1152 * 8) : "memory");
      if (all_lanes((flag_ok(g0, tag, 1) || voffF == j * 8) && ((g1[0] ^ gl) >> 16 | (g1[1] ^ gh)) == 0u)) break;
      __builtin_amdgcn_s_sleep(1);
    }
    STAMP(19);
  }
  sfor<0, 3>([&](auto C) AI { sfor<0, 4>([&](auto P) AI { issueX(C, P); }); });

  const int trb = h * 256 + q4 * 64 + blk * 32;
  unsigned aWq = lds0 + trb + (((cp & 1) << 1) | (cp >> 1)) * 8 + hd * 2048;
  PIN(aWq);
  AttnState st;
  {
    f32x16 acc[4];
#pragma unroll
    for (int t = 0; t < 4; ++t) acc[t] = f32x16{};
    auto stage = [&](auto Nc) AI {
      constexpr int n = decltype(Nc)::value, ks = n >> 2, T = n & 3, C = n >> 4, ksl = ks & 3;
      if constexpr ((n & 15) == 0) {
        if constexpr (C > 0) bump(aWq, ((C & 3) - ((C - 1) & 3)) * A_SLOT);
        constexpr int NV = C < 2 ? 8 : C == 2 ? 12 : (C + 1 < 9 ? 8 : 4) + (C + 2 < 9 ? 8 : 4);
        asm volatile("s_waitcnt vmcnt(%4) lgkmcnt(0)\n\ts_barrier" : "+v"(X[4 * C]), "+v"(X[4 * C + 1]), "+v"(X[4 * C + 2]), "+v"(X[4 * C + 3]) : "i"(NV) : "memory");
      }
      st.F[n & 7] = trfrag(lp(aWq) + ((2 * ksl) * 8 + T) * 512, lp(aWq) + ((2 * ksl + 1) * 8 + T) * 512);
      if constexpr (n & 1) {
        if constexpr (n & 2) { if constexpr (C > 0) issue(std::integral_constant<int, C + 3>{}, std::integral_constant<int, ((n & 15) >> 2)>{}); }
        else issueX(std::integral_constant<int, C + 3>{}, std::integral_constant<int, ((n & 15) >> 2)>{});
      }
    };
    sfor<0, 4>([&](auto N) AI { stage(N); SBAR(); });
    STAMP(1);
    sfor<0, 144>([&](auto Mc) AI {
      constexpr int m = decltype(Mc)::value;
      if constexpr (m + 4 < 144) stage(std::integral_constant<int, m + 4>{});
      acc[m & 3] = MFMA(st.F[m & 7], asfrag(X[m >> 2]), acc[m & 3]);
      SBAR();
    });
#pragma unroll
    for (int t = 0; t < 4; ++t) { st.Q[2 * t] = asfrag(pack8(acc[t], 0)); st.Q[2 * t + 1] = asfrag(pack8(acc[t], 1)); }
  }
  STAMP(2);
  unsigned aK0 = lds0 + (9 & 3) * A_SLOT + 2048 * (r32 >> 3) + 64 * (r32 & 7) + 16 * ((0 + h) ^ ((r32 >> 2) & 3));
  unsigned aK1 = aK0 ^ 32, aV = lds0 + (9 & 3) * A_SLOT + trb + cp * 8;
  PIN(aK0); PIN(aK1); PIN(aV);
#pragma unroll
  for (int t = 0; t < 4; ++t) st.O[t] = f32x16{};
  auto kfrag = [&](int off, auto Ic) AI { constexpr int i = decltype(Ic)::value; return ldfrag(lp((i & 1) ? aK1 : aK0) + off + 512 * (i >> 1)); };
  auto vfrag = [&](int off, auto Ic) AI { constexpr int i = decltype(Ic)::value, ks = i >> 2, T = i & 3;
    return trfrag(lp(aV) + off + ((2 * ks) * 4 + T) * 512, lp(aV) + off + ((2 * ks + 1) * 4 + T) * 512); };
  auto step = [&](auto Par, auto HQK, auto HPV, auto NXT, auto NVc, int u, int ko, int vo, int kn, int dsl, auto&& dmaf) AI {
    constexpr int par = decltype(Par)::value, nxt = decltype(NXT)::value, NV = decltype(NVc)::value;
    constexpr bool hqk = decltype(HQK)::value, hpv = decltype(HPV)::value;
    float l = st.l, eprev = 0.f;
    sfor<0, 16>([&](auto Mc) AI {
      constexpr int m = decltype(Mc)::value, n = m + 4;
      if constexpr (n < 8) { if constexpr (hqk) st.F[n & 7] = kfrag(ko, std::integral_constant<int, n>{}); }
      else if constexpr (n < 16) { if constexpr (hpv) st.F[n & 7] = vfrag(vo, std::integral_constant<int, n - 8>{}); }
      else {
        if constexpr (n == 16 && NV >= 0) { bump(aK0, dsl); bump(aK1, dsl); bump(aV, dsl); wait_bar<NV>(); }
        if constexpr (nxt == 1) st.F[n & 7] = kfrag(kn, std::integral_constant<int, n - 16>{});
      }
      dmaf(Mc);
      if constexpr (m < 8) { if constexpr (hqk) st.S[par ^ 1] = MFMA(st.F[m & 7], st.Q[m], m == 0 ? st.negm : st.S[par ^ 1]); }
      else { if constexpr (hpv) st.O[m & 3] = MFMA(st.F[m & 7], asfrag(st.Pk[par ^ 1][(m - 8) >> 2]), st.O[m & 3]); }
      const float e = __builtin_amdgcn_exp2f(st.S[par][m]);
      l += e; PIN(l);
      if constexpr (m & 1) { st.Pk[par][m >> 3][(m >> 1) & 3] = cvtpk(eprev, e); PIN(st.Pk[par][m >> 3]); }
      else { eprev = e; PIN(eprev); }
      SBAR();
    });
    st.l = l;
  };
  using I0 = std::integral_constant<int, 0>; using I1 = std::integral_constant<int, 1>; using IM = std::integral_constant<int, -1>;
  using BT = std::true_type; using BF = std::false_type;
  {
    wait_bar<8>();
    sfor<0, 4>([&](auto P) AI { issue(std::integral_constant<int, 12>{}, P); });
    f32x16 s = f32x16{};
    sfor<0, 8>([&](auto I) AI { s = MFMA(kfrag(2 * 8192, I), st.Q[decltype(I)::value], s); });
    sfor<0, 4>([&](auto I) AI { st.F[decltype(I)::value] = kfrag(0, I); });
    float mt = s[0];
#pragma unroll
    for (int r = 1; r < 16; ++r) mt = __builtin_fmaxf(mt, s[r]);
    mt = swap_max(mt) + M_MARGIN;
#pragma unroll
    for (int r = 0; r < 16; ++r) { st.S[0][r] = s[r] - mt; st.negm[r] = -mt; }
    PIN(st.negm);
    st.l = 0.f;
    SBAR();
  }
  auto nodma = [&](auto) AI {};
  auto warmF = [&](auto Uc) AI {
    constexpr int u = decltype(Uc)::value;
    if constexpr (u < 12) { if (w == (u & 7)) {
        unsigned t = lane; PIN(t); t += (u % 3) * 64;
        if constexpr (u < 3) touch(lds0 + A_JUNK, (256 * j + 16 * rb) * 1152 + (t < 144 ? t : 0) * 128, rWo);
        else if constexpr (u < 9) { const unsigned rr = t / 5, cc = t - rr * 5;
          touch(lds0 + A_JUNK, (36 * rb + (rr < 36 ? rr : 0)) * (F_ * 2) + ((576 * j) & ~127u) + cc * 128, mkrsrc(u < 6 ? wg : wu, D_ * F_ * 2)); }
        else touch(lds0 + A_JUNK, (288 * j + 18 * rb) * 1152 + (t < 162 ? t : 0) * 128, mkrsrc(wd, F_ * D_ * 2));
    } }
  };
  sfor<0, 8>([&](auto Ic) AI {
    constexpr int i = decltype(Ic)::value, C = 9 + i, dsl = (((C + 1) & 3) - (C & 3)) * A_SLOT;
    auto dmaf = [&](auto Mc) AI { constexpr int m = decltype(Mc)::value;
      if constexpr (i > 0 && (m & 3) == 1) issue(std::integral_constant<int, C + 3>{}, std::integral_constant<int, (m >> 2)>{});
      if constexpr (m == 15) warmF(std::integral_constant<int, 2 * i>{}); };
    if constexpr (i == 0) step(I0{}, BT{}, BF{}, I1{}, IM{}, 0, 0, 0, 8192, 0, dmaf);
    else if constexpr (i < 7) step(I0{}, BT{}, BT{}, I1{}, IM{}, 2 * i, 0, 2 * 8192, 8192, 0, dmaf);
    else step(I0{}, BT{}, BT{}, I0{}, IM{}, 2 * i, 0, 2 * 8192, 0, 0, dmaf);
    auto warm = [&](auto Mc) AI { if constexpr (decltype(Mc)::value == 6) warmF(std::integral_constant<int, 2 * i + 1>{}); };
    if constexpr (i < 7) step(I1{}, BT{}, BT{}, I1{}, std::integral_constant<int, 8>{}, 2 * i + 1, 8192, 3 * 8192, 0, dsl, warm);
    else step(I1{}, BF{}, BT{}, I0{}, IM{}, 15, 0, 3 * 8192, 0, 0, nodma);
  });
  bf16x8 Of[16];
  {
    constexpr int s16 = (16 & 3) * A_SLOT;
    sfor<0, 8>([&](auto Ic) AI { constexpr int i = decltype(Ic)::value; st.O[i & 3] = MFMA(vfrag(8192, Ic), asfrag(st.Pk[1][i >> 2]), st.O[i & 3]); });
    STAMP(3);
    wait_bar<8>();
    if (__builtin_expect(__builtin_amdgcn_ballot_w64(!(st.l <= L_LIMIT)) != 0, 0)) {
      const u16* kg = kc + ((size_t)j * SK + (SK - WIN) + r32) * DH + 8 * h;
      const u16* vg = vc + ((size_t)j * SK + (SK - WIN) + 4 * h) * DH + r32;
      float mt = -__builtin_inff();
#pragma unroll 1
      for (int u = 0; u < 16; ++u) {
        f32x16 s = f32x16{};
#pragma unroll
        for (int i = 0; i < 8; ++i) s = MFMA(*(const bf16x8*)(kg + (size_t)u * 32 * DH + 16 * i), st.Q[i], s);
#pragma unroll
        for (int r = 0; r < 16; ++r) mt = __builtin_fmaxf(mt, s[r]);
      }
      mt = swap_max(mt);
      float l = 0.f;
#pragma unroll
      for (int t = 0; t < 4; ++t) st.O[t] = f32x16{};
#pragma unroll 1
      for (int u = 0; u < 16; ++u) {
        f32x16 s = f32x16{};
#pragma unroll
        for (int i = 0; i < 8; ++i) s = MFMA(*(const bf16x8*)(kg + (size_t)u * 32 * DH + 16 * i), st.Q[i], s);
#pragma unroll
        for (int r = 0; r < 16; ++r) { s[r] = __builtin_amdgcn_exp2f(s[r] - mt); l += s[r]; }
#pragma unroll
        for (int ks = 0; ks < 2; ++ks) {
          const bf16x8 pf = asfrag(pack8(s, ks));
#pragma unroll
          for (int t = 0; t < 4; ++t) {
            bf16x8 vf;
#pragma unroll
            for (int e = 0; e < 8; ++e) vf[e] = (short)vg[(size_t)(u * 32 + 16 * ks + 8 * (e >> 2) + (e & 3)) * DH + 32 * t];
            st.O[t] = MFMA(vf, pf, st.O[t]);
          }
        }
      }
      st.l = l;
    }
    const float inv = __builtin_amdgcn_rcpf(swap_add(st.l));
#pragma unroll
    for (int t = 0; t < 4; ++t) {
#pragma unroll
      for (int r = 0; r < 16; ++r) st.O[t][r] *= inv;
      Of[2 * t] = asfrag(pack8(st.O[t], 0)); Of[2 * t + 1] = asfrag(pack8(st.O[t], 1));
    }
    const lptr xw = L + (hd ? s16 : A_SPARE) + g * 8192 + lane * 16, xr = L + (hd ? A_SPARE : s16) + g * 8192 + lane * 16;
#pragma unroll
    for (int i = 0; i < 8; ++i) *(LAS bf16x8*)(xw + i * 1024) = Of[i];
    wait_bar<8>();
#pragma unroll
    for (int i = 0; i < 8; ++i) Of[8 + i] = ldfrag(xr + i * 1024);
    asm volatile("s_waitcnt lgkmcnt(0)\n\ts_barrier" ::: "memory");
    SBAR();
  }
  STAMP(4);
  const unsigned hf = hd;
  const rsrc_t rGU = mkrsrc(hf ? wu : wg, D_ * F_ * 2), rWd = mkrsrc(wd, F_ * D_ * 2), rH2 = mkrsrc(ws + WS_H2, S_ * D_ * 2);
  unsigned lane2 = lane; PIN(lane2);
  const int h_ = lane2 >> 5, r8_ = (lane2 >> 2) & 7, cp_ = lane2 & 3;
  unsigned voffGU[5];
#pragma unroll
  for (int p = 0; p < 5; ++p) {
    const int piece = p < 4 ? (int)g + 4 * p : 16 + (int)(g & 1), stl = 2 * piece + h_, R = stl / 9, sub = stl - 9 * R;
    voffGU[p] = (8 * R + r8_) * (F_ * 2) + 576 * j + sub * 64 + ((cp_ ^ (hf ? 2 : 0)) * 16);
  }
  const unsigned m0gu = lds0 + hf * 18432 + g * 1024, m0gux = lds0 + hf * 18432 + (16 + (g & 1)) * 1024;
  const bool xgu = (g >> 1) != 0;
  unsigned voffWd = 0;
  auto issueF = [&](auto Cc, auto Pc) AI {
    constexpr int C = decltype(Cc)::value, P = decltype(Pc)::value, slot = bslot(C);
    if constexpr (C < 18) {
      if constexpr (P < 4) dma16<0>(m0gu + slot + P * 4096, voffGU[P], rGU, C * 32 * F_ * 2);
      else { if (xgu == (bool)(C & 1)) dma16<0>(m0gux + slot, voffGU[4], rGU, C * 32 * F_ * 2); }
    } else if constexpr (C < 27) {
      if constexpr (P < 4) dma16<0>(m0w1 + slot + P * 8192, voffWd, rWd, (C - 18) * 128 + P * 64 * 1152);
      else { if (xgu == (bool)(C & 1)) {
          const unsigned e = (g & 1) + 2 * hf;
          dma16<0>(lds0 + (32 + e) * 1024 + slot, voffWd, rWd, (C - 18) * 128 + ((32 + e) - w) * 8 * 1152); } }
    }
  };
  const rsrc_t rP = mkrsrc(ws + WS_P, 16 * ACT_B);
  auto project = [&](auto NKc, auto CBc, const bf16x8* Bf, unsigned aOwn, unsigned aPar, auto&& dslot, auto&& nv, auto&& dma, auto&& preload, auto NXc, auto&& extra, float (&y)[9]) AI {
    constexpr int NK = decltype(NKc)::value, HK = NK / 2, CB = decltype(CBc)::value, NX = decltype(NXc)::value, NA = XNA;
    unsigned ln = lane; PIN(ln);
    const unsigned cons = 4 * g + ((ln & 31) >> 3);
    const unsigned voffP = rb * 16 * 147456 + (CB == 0 ? cons * 147456 + j * 128 : j * 147456 + cons * 128) + (4 * hd + (ln >> 5)) * 2048 + (ln & 7) * 16;
    const unsigned vo = rb * 16 * 147456 + w * 2048 + (CB == 0 ? j * 147456 + ln * 16 : (ln >> 3) * 147456 + j * 128 + (ln & 7) * 16);
    const unsigned co = rb * 128 + CB * 2048;
    PIN(aOwn); PIN(aPar);
    bf16x8 F[8]; f32x16 acc[2]; u32x4 pv[18]; u32x2 pl;
    const unsigned vf = (ln & 15) * 8;
    auto ldp = [&](auto Tc) AI { constexpr int t = decltype(Tc)::value; gld16c<0>(pv[t], vo, rP, (t >> 1) * 16384 + (t & 1) * (CB == 0 ? 1024 : 8 * 147456)); };
    auto red = [&](auto Tc) AI {
      constexpr int t = decltype(Tc)::value;
      const bool b3 = (ln >> 3) & 1;
      float sm[8];
#pragma unroll
      for (int e = 0; e < 4; ++e) { sm[2 * e] = bflo(pv[2 * t][e]) + bflo(pv[2 * t + 1][e]); sm[2 * e + 1] = bfhi(pv[2 * t][e]) + bfhi(pv[2 * t + 1][e]); }
      const float a0 = swap32_add(sm[0], sm[4]), a1 = swap32_add(sm[1], sm[5]), a2 = swap32_add(sm[2], sm[6]), a3 = swap32_add(sm[3], sm[7]);
      const float b0 = swap16_add(a0, a2), b1 = swap16_add(a1, a3);
      y[t] = (b3 ? b1 : b0) + ror8(b3 ? b0 : b1);
    };
    auto stage = [&](auto Nc) AI {
      constexpr int n = decltype(Nc)::value, i = n / NK, ks = n % NK;
      if constexpr (ks == 0 && i > 0) {
        bump(aOwn, dslot(i)); bump(aPar, dslot(i));
        constexpr int NV = decltype(nv(std::integral_constant<int, i>{}))::value;
        if constexpr (NA > 0 && i == NA + 5) {
          asm volatile("s_waitcnt vmcnt(%1)" : "+v"(pl) : "i"(NV) : "memory");
          poll_until(pl, vf, rC, co, tag, NA, j * 8);
          lbar();
        } else wait_bar<NV>();
        if constexpr ((NA > 0 && i == NA + 2) || i == 8) signal(rC, co + j * 8, tag, i - 2, tid);
        if constexpr (NA > 0 && i == NA + 4) gld8c(pl, vf, rC, co);
      }
      const lptr b = lp(ks < HK ? aOwn : aPar) + (2 * (ks % HK)) * 1024;
      F[n & 7] = trfrag(b, b + 1024);
      if constexpr (NA > 0 && i == NA + 5 && ks < 2 * NA) ldp(std::integral_constant<int, ks>{});
      dma(std::integral_constant<int, i>{}, std::integral_constant<int, ks>{});
    };
    sfor<0, 4>([&](auto N) AI { stage(N); SBAR(); });
    sfor<0, 9 * NK>([&](auto Mc) AI {
      constexpr int m = decltype(Mc)::value, i = m / NK, ks = m % NK;
      if constexpr (m + 4 < 9 * NK) stage(std::integral_constant<int, m + 4>{});
      if constexpr (ks == 0) acc[i & 1] = MFMA(F[m & 7], Bf[0], f32x16{});
      else acc[i & 1] = MFMA(F[m & 7], Bf[ks], acc[i & 1]);
      if constexpr (i > 0 && (ks == 3 || ks == 5)) { constexpr int s = (ks - 3) >> 1;
        __builtin_amdgcn_raw_buffer_store_b128(pack8(acc[(i - 1) & 1], s), rP, voffP, (i - 1) * 16384 + s * 4096, 16); }
      SBAR();
    });
    __builtin_amdgcn_raw_buffer_store_b128(pack8(acc[0], 0), rP, voffP, 8 * 16384, 16);
    __builtin_amdgcn_raw_buffer_store_b128(pack8(acc[0], 1), rP, voffP, 8 * 16384 + 4096, 16);
    STAMP(CB == 0 ? 5 : 12);
    gld8c(pl, vf, rC, co);
    asm volatile("s_waitcnt vmcnt(0) lgkmcnt(0)\n\ts_barrier" : "+v"(pl) :: "memory");
    if constexpr (CB == 0) STAMP(30);
    signal(rC, co + j * 8, tag, 9, tid);
#pragma unroll
    for (int t = 0; t < 2 * NA; ++t) PIN(pv[t]);
    poll_until(pl, vf, rC, co, tag, 6, j * 8);
    sfor<2 * NA, 12>(ldp);
    gld8c(pl, vf, rC, co);
    sfor<0, NA>(red);
    preload();
    asm volatile("s_waitcnt vmcnt(0)" : "+v"(pl) :: "memory");
#pragma unroll
    for (int t = 2 * NA; t < 12; ++t) PIN(pv[t]);
    poll_until(pl, vf, rC, co, tag, 9, j * 8);
    STAMP(CB == 0 ? 6 : 13);
    sfor<12, 18>(ldp);
    extra();
    sfor<NA, 6>(red);
    sfor<6, 9>([&](auto Tc) AI {
      constexpr int t = decltype(Tc)::value;
      asm volatile("s_waitcnt vmcnt(%2)" : "+v"(pv[2 * t]), "+v"(pv[2 * t + 1]) : "i"(16 - 2 * t + NX) : "memory");
      red(Tc);
    });
  };
  auto rowsum = [&](float v, int slot) AI {
    v += ror8(v); v = swap16_add(v, v); v = swap32_add(v, v);
    if (lane < 8) *(LAS float*)(L + SP_RED + slot * 256 + lane * 32 + w * 4) = v;
    lbar();
    const f32x4 r0 = *(LAS f32x4*)(L + SP_RED + slot * 256 + (lane & 7) * 32), r1 = *(LAS f32x4*)(L + SP_RED + slot * 256 + (lane & 7) * 32 + 16);
    return ((r0[0] + r0[1]) + (r0[2] + r0[3])) + ((r1[0] + r1[1]) + (r1[2] + r1[3]));
  };
  float x2[9];
  {
    float y[9], ga[9], sb[9], cb[9]; u32x4 tbv;
    const unsigned aOwn = lds0 + (17 & 3) * A_SLOT + trb + cp * 8 + hd * 512 + hd * 16384;
    project(std::integral_constant<int, 16>{}, I0{}, Of, aOwn, aOwn ^ 16384,
            [](int i) AI { return (((17 + i) & 3) - ((16 + i) & 3)) * A_SLOT; },
            [](auto Ic) AI { constexpr int i = decltype(Ic)::value;
              constexpr int ops[9] = {4, 6, 6, 6, 6, 6, 4, 6, 2}, NA = XNA;
              constexpr int prev = ops[i - 1] + (NA > 0 && i - 1 == NA + 5 ? 2 * NA : 0);
              return std::integral_constant<int, i == 1 ? 8 : (NA > 0 && i == NA + 5) || i == 8 ? prev : (NA > 0 && i == NA + 2) ? prev + 1 : prev + ops[i > 1 ? i - 2 : 0]>{}; },
            [&](auto Ic, auto Kc) AI {
              constexpr int C = 17 + decltype(Ic)::value, ks = decltype(Kc)::value;
              if constexpr (C < 23) { if constexpr ((ks & 3) == 1) issue(std::integral_constant<int, C + 3>{}, std::integral_constant<int, (ks >> 2)>{}); }
              else if constexpr (C == 23) {
                if constexpr (ks == 1) dma16<0>(m0w1 + SP_X, lane2 * 16 + w * 1024, rXr, (rb * 128 + j * 8) * 1152);
                if constexpr (ks == 5) gld16c<0>(tbv, (w < 5 ? w * 64 + lane2 : 0) * 16, rC, WS_TABB);
                if constexpr (ks == 13) { if (w == 0) dma16<0>(lds0 + SP_X + 8192, lane2 * 16, rXr, (rb * 128 + j * 8) * 1152 + 8192); }
              } else if constexpr (C == 24) { if constexpr (ks % 3 == 1) issueF(I0{}, std::integral_constant<int, ks / 3>{}); }
              else if constexpr (ks == 8) {
                PIN(tbv);
                if (w < 5) *(LAS u32x4*)(L + B_SPARE + (w * 64 + lane2) * 16) = tbv;
              }
            },
            [&]() AI {
              const int e = lane2 >> 3, n0 = 16 * (w >> 1) + 4 * (w & 1) + 8 * (e >> 2) + (e & 3);
              const lptr tb = L + B_SPARE + n0 * 2, xr = L + SP_X + (lane2 & 7) * 1152 + n0 * 2;
#pragma unroll
              for (int t = 0; t < 9; ++t) {
                x2[t] = bflo(*(LAS u16*)(xr + t * 128)); ga[t] = bflo(*(LAS u16*)(tb + t * 128));
                sb[t] = bflo(*(LAS u16*)(tb + 1152 + t * 128)); cb[t] = bflo(*(LAS u16*)(tb + 2 * 1152 + t * 128));
                PIN(x2[t]); PIN(ga[t]); PIN(sb[t]); PIN(cb[t]);
              }
            },
            std::integral_constant<int, 9>{},
            [&]() AI { sfor<0, 5>([&](auto P) AI { issueF(I1{}, P); }); sfor<0, 5>([&](auto P) AI { issueF(std::integral_constant<int, 2>{}, P); }); }, y);
    const int e = lane >> 3, row8 = lane & 7;
    float s = 0.f;
#pragma unroll
    for (int t = 0; t < 9; ++t) { x2[t] += ga[t] * y[t]; s += x2[t]; }
    const float mean = rowsum(s, 0) * (1.f / D_);
    float q = 0.f;
#pragma unroll
    for (int t = 0; t < 9; ++t) { const float d = x2[t] - mean; q += d * d; }
    const float rstd = rsqrtf(rowsum(q, 1) * (1.f / D_) + LN_EPS);
    const lptr sc = L + bslot(3) + w * 1152;
#pragma unroll
    for (int t = 0; t < 9; ++t) {
      const float hv = (x2[t] - mean) * rstd * cb[t] + sb[t];
      *(LAS u16*)(sc + t * 128 + row8 * 16 + e * 2) = (u16)cvtpk(hv, hv);
    }
    asm volatile("s_waitcnt lgkmcnt(0)" ::: "memory");
    const u32x4 c0 = *(LAS u32x4*)(sc + lane * 16), c1 = *(LAS u32x4*)(sc + 1024 + row8 * 16);
    const unsigned ho = (((rb * 4 + (j >> 2)) * NKS + (w >> 1)) * 64 + (w & 1) * 32 + (j & 3) * 8 + row8) * 16;
    __builtin_amdgcn_raw_buffer_store_b128(c0, rH2, ho + e * 4096, 0, 16);
    if (lane < 8) __builtin_amdgcn_raw_buffer_store_b128(c1, rH2, ho + 8 * 4096, 0, 16);
    STAMP(7);
    asm volatile("s_waitcnt vmcnt(0)\n\ts_barrier" ::: "memory");
    signal(rC, 3 * 2048 + rb * 128 + j * 8, tag, 1, tid);
    poll_until(u32x2{0u, ~tag[1]}, (lane & 15) * 8, rC, 3 * 2048 + rb * 128, tag, 1, j * 8);
    STAMP(8);
  }
  bf16x8 F[8], Af[18];
  unsigned lane3 = lane; PIN(lane3);
  const int trb3 = (lane3 >> 5) * 256 + ((lane3 >> 2) & 3) * 64 + (lane3 & 3) * 8, blk3 = (lane3 >> 4) & 1;
  {
    const unsigned voffH = lane3 * 16 + rg * (NKS * 1024);
    u32x4 H[NKS];
    auto issueH = [&](auto Cc, auto Pc) AI {
      constexpr int C = decltype(Cc)::value, P = decltype(Pc)::value;
      if constexpr (C < 18) gld16c<1024 * P>(H[2 * C + P], voffH, rH2, C * 2048);
    };
    sfor<0, 3>([&](auto C) AI { sfor<0, 2>([&](auto P) AI { issueH(C, P); }); });
    unsigned aE = lds0 + bslot(0) + trb3 + blk3 * 18432 + hf * 2048 + ((hf ^ blk3) * 32);
    unsigned aO = lds0 + bslot(0) + trb3 + blk3 * 18432 + hf * 2560 + ((1 ^ hf ^ blk3) * 32);
    PIN(aE); PIN(aO);
    voffWd = (288 * j + 8 * w + ((lane3 >> 2) & 7)) * 1152 + (lane3 >> 5) * 64 + (lane3 & 3) * 16;
    f32x16 acc[9];
#pragma unroll
    for (int t = 0; t < 9; ++t) acc[t] = f32x16{};
    auto stage = [&](auto Nc) AI {
      constexpr int n = decltype(Nc)::value, ks = n / 9, T = n % 9, C = n / 18, r = n % 18, ksl = ks & 1;
      if constexpr (r == 0) {
        if constexpr (C > 0) { bump(aE, bslot(C) - bslot(C - 1)); bump(aO, bslot(C) - bslot(C - 1)); }
        constexpr int NV = C == 0 ? 4 : C == 1 ? 8 : 9 + (C + 1 < 18 ? 2 : 0) + (C + 2 < 18 ? 2 : 0);
        asm volatile("s_waitcnt vmcnt(%2) lgkmcnt(0)\n\ts_barrier" : "+v"(H[2 * C]), "+v"(H[2 * C + 1]) : "i"(NV) : "memory");
      }
      const lptr b = lp((T & 1) ? aO : aE) + ((2 * ksl) * 9 + (T >> 1)) * 512;
      F[n & 7] = trfrag(b, b + 9 * 512);
      if constexpr (C + 3 != 20) {
        if constexpr (r == 0 || r == 2) issueH(std::integral_constant<int, C + 3>{}, std::integral_constant<int, (r >> 1)>{});
        if constexpr (r >= 4 && r < 14 && (r & 1) == 0) issueF(std::integral_constant<int, C + 3>{}, std::integral_constant<int, ((r - 4) >> 1)>{});
      }
    };
    sfor<0, 4>([&](auto N) AI { stage(N); SBAR(); });
    STAMP(9);
    sfor<0, 324>([&](auto Mc) AI {
      constexpr int m = decltype(Mc)::value;
      if constexpr (m + 4 < 324) stage(std::integral_constant<int, m + 4>{});
      acc[m % 9] = MFMA(F[m & 7], asfrag(H[m / 9]), acc[m % 9]);
      SBAR();
    });
    STAMP(10);
    lbar();
    const lptr xw = L + bslot(16) + w * 9216 + lane3 * 16, xr = L + bslot(16) + (w ^ 4) * 9216 + lane3 * 16;
#pragma unroll
    for (int t = 0; t < 9; ++t) {
      float a[8];
#pragma unroll
      for (int e = 0; e < 8; ++e) {
        const float gv = acc[t][e];
        a[e] = gv * acc[t][8 + e] * __builtin_amdgcn_rcpf(1.f + __builtin_amdgcn_exp2f(gv * -1.4426950408889634f));
      }
      u32x4 pk = {cvtpk(a[0], a[1]), cvtpk(a[2], a[3]), cvtpk(a[4], a[5]), cvtpk(a[6], a[7])};
      Af[t] = asfrag(pk);
      *(LAS bf16x8*)(xw + t * 1024) = Af[t];
    }
    wait_bar<0>();
#pragma unroll
    for (int t = 0; t < 9; ++t) Af[9 + t] = ldfrag(xr + t * 1024);
    lbar();
    SBAR();
    sfor<0, 5>([&](auto P) AI { issueF(std::integral_constant<int, 20>{}, P); });
  }
  STAMP(11);
  {
    float y[9], gb[9];
    const unsigned aT = lds0 + bslot(18) + trb3 + blk3 * 32 + hf * 512;
    project(std::integral_constant<int, 18>{}, std::integral_constant<int, 4>{}, Af, aT + hf * 18432, aT + (1 - hf) * 18432,
            [](int i) AI { return bslot(18 + i) - bslot(17 + i); },
            [](auto Ic) AI { constexpr int i = decltype(Ic)::value;
              constexpr int ops[9] = {4, 6, 6, 6, 6, 6, 2, 2, 2}, NA = XNA;
              constexpr int prev = ops[i - 1] + (NA > 0 && i - 1 == NA + 5 ? 2 * NA : 0);
              return std::integral_constant<int, i == 1 ? 8 : (NA > 0 && i == NA + 5) || i == 8 ? prev : (NA > 0 && i == NA + 2) ? prev + 1 : prev + ops[i > 1 ? i - 2 : 0]>{}; },
            [&](auto Ic, auto Kc) AI {
              constexpr int C = 18 + decltype(Ic)::value, ks = decltype(Kc)::value;
              if constexpr (ks >= 1 && ks < 16 && (ks % 3) == 1) issueF(std::integral_constant<int, C + 3>{}, std::integral_constant<int, (ks / 3)>{});
            },
            [&]() AI {
              const int e = lane3 >> 3, n0 = 16 * (w >> 1) + 4 * (w & 1) + 8 * (e >> 2) + (e & 3);
#pragma unroll
              for (int t = 0; t < 9; ++t) { gb[t] = bflo(*(LAS u16*)(L + B_SPARE + 3 * 1152 + n0 * 2 + t * 128)); PIN(gb[t]); }
            },
            I0{}, [&]() AI {}, y);
    unsigned ln = lane; PIN(ln);
    const int e = ln >> 3, row8 = ln & 7, n0 = 16 * (w >> 1) + 4 * (w & 1) + 8 * (e >> 2) + (e & 3);
    const lptr im = L + row8 * 1152 + n0 * 2;
#pragma unroll
    for (int t = 0; t < 9; ++t) {
      const float ov = x2[t] + gb[t] * y[t];
      *(LAS u16*)(im + t * 128) = (u16)cvtpk(ov, ov);
    }
    lbar();
    u16* op = out + (size_t)(rb * 128 + j * 8) * D_;
    const unsigned t2 = w * 64 + ln;
    *(u32x4*)(op + t2 * 8) = *(LAS u32x4*)(L + t2 * 16);
    if (w == 0) *(u32x4*)(op + 4096 + ln * 8) = *(LAS u32x4*)(L + 8192 + ln * 16);
  }
  if (blockIdx.x == 0 && w == 1) {
#pragma unroll 1
    for (int q = 0; q < 4; ++q) poll_until(u32x2{0u, ~tag[1]}, lane * 8, rC, 7 * 2048 + q * 512, tag, 1, ~0u);
    if (lane == 0) __builtin_amdgcn_raw_buffer_store_b64(tag, rC, 0, WS_EPOCH, 16);
  }
  asm volatile(".Lkm_end:");
  STAMP(14);
}

extern "C" void kernel_launch(void* const* d_in, const int* in_sizes, int n_in, void* d_out, int out_size, void* d_ws, size_t ws_size,
                              hipStream_t stream) {
  (void)in_sizes; (void)n_in; (void)out_size; (void)ws_size;
  const u16* x = (const u16*)d_in[0]; const u16* kc = (const u16*)d_in[1]; const u16* vc = (const u16*)d_in[2];
  const u16* wq = (const u16*)d_in[3]; const u16* wo = (const u16*)d_in[4];
  const u16* wg = (const u16*)d_in[6]; const u16* wu = (const u16*)d_in[7]; const u16* wd = (const u16*)d_in[8];
  const u16* c = (const u16*)d_in[9]; const u16* wmod = (const u16*)d_in[10];
  char* ws = (char*)d_ws;
  (void)hipFuncSetAttribute((const void*)kmain, hipFuncAttributeMaxDynamicSharedMemorySize, A_LDS);
  kmain<<<256, 512, A_LDS, stream>>>(x, wq, kc, vc, wo, wg, wu, wd, c, wmod, ws, (u16*)d_out);
}
```

```cpp
#include <hip/hip_runtime.h>
#include <stdint.h>
#include <type_traits>

typedef short    bf16x8 __attribute__((ext_vector_type(8)));
typedef short    s16x4  __attribute__((ext_vector_type(4)));
typedef float    f32x16 __attribute__((ext_vector_type(16)));
typedef float    f32x4  __attribute__((ext_vector_type(4)));
typedef float    f32x2  __attribute__((ext_vector_type(2)));
typedef __bf16   bf16x2 __attribute__((ext_vector_type(2)));
typedef unsigned u32x4  __attribute__((ext_vector_type(4)));
typedef unsigned u32x2  __attribute__((ext_vector_type(2)));
typedef unsigned short u16;
#define LAS __attribute__((address_space(3)))
typedef LAS char* lptr;
typedef __amdgpu_buffer_rsrc_t rsrc_t;
#define DI __device__ __forceinline__
#define AI __attribute__((always_inline))
#define SBAR() __builtin_amdgcn_sched_barrier(0)
#define PIN(x) asm volatile("" : "+v"(x))
#define MFMA(a, b, c) __builtin_amdgcn_mfma_f32_32x32x16_bf16((a), (b), (c), 0, 0, 0)

constexpr int S_ = 2048, D_ = 576, HQ = 32, HKV = 16, SK = 2048, DH = 128, WIN = 512, F_ = 4608;
constexpr int NKS = D_ / 16;
constexpr int NRG = S_ / 32;
constexpr float LN_EPS = 1e-5f, LOG2E = 1.4426950408889634f;
constexpr float QSCALE = 0.08838834764831845f * 1.4426950408889634f;

constexpr unsigned WS_FLAG = 0;
constexpr unsigned WS_EPOCH = 16384;
constexpr unsigned WS_GRAN = 20480;
constexpr unsigned WS_TABB = 49152;
constexpr size_t WS_XH   = 65536;
constexpr size_t ACT_B   = (size_t)S_ * D_ * 2;
constexpr size_t WS_H2   = WS_XH + ACT_B;
constexpr size_t WS_X2   = WS_H2 + ACT_B;
constexpr size_t WS_P    = WS_X2 + 2 * ACT_B;
constexpr size_t WS_P2   = WS_P;
constexpr size_t WS_DBG  = WS_P + 32 * ACT_B;

DI unsigned rfl(unsigned x) { return __builtin_amdgcn_readfirstlane(x); }
DI unsigned cvtpk(float lo, float hi) { f32x2 v = {lo, hi}; bf16x2 b = __builtin_convertvector(v, bf16x2); return __builtin_bit_cast(unsigned, b); }
DI float bflo(unsigned w) { return __uint_as_float(w << 16); }
DI float bfhi(unsigned w) { return __uint_as_float(w & 0xffff0000u); }
DI float bfr(float x) { return bflo(cvtpk(x, x)); }
DI rsrc_t mkrsrc(const void* p, unsigned bytes) { return __builtin_amdgcn_make_buffer_rsrc((void*)p, (short)0, (int)bytes, 0x00020000); }
template <int OFF> DI void dma16(unsigned m0v, unsigned voff, rsrc_t r, unsigned soff) {
  asm volatile("s_mov_b32 m0, %0\n\ts_nop 0\n\tbuffer_load_dwordx4 %1, %2, %3 offen offset:%4 lds"
               :: "s"(m0v), "v"(voff), "s"(r), "s"(soff), "i"(OFF) : "memory");
}
DI void touch(unsigned m0v, unsigned voff, rsrc_t r) {
  asm volatile("s_mov_b32 m0, %0\n\ts_nop 0\n\tbuffer_load_dword %1, %2, 0 offen lds" :: "s"(m0v), "v"(voff), "s"(r) : "memory");
}
template <int OFF> DI void gld16(u32x4& d, unsigned voff, rsrc_t r, unsigned soff) {
  asm volatile("buffer_load_dwordx4 %0, %1, %2, %3 offen offset:%4" : "=v"(d) : "v"(voff), "s"(r), "s"(soff), "i"(OFF) : "memory");
}
template <int OFF> DI void gld16c(u32x4& d, unsigned voff, rsrc_t r, unsigned soff) {
  asm volatile("buffer_load_dwordx4 %0, %1, %2, %3 offen offset:%4 sc1" : "=v"(d) : "v"(voff), "s"(r), "s"(soff), "i"(OFF) : "memory");
}
template <bool FIRST> DI void mfma16(f32x4& c, bf16x8 a, u32x4 b) {
  if constexpr (FIRST) asm volatile("v_mfma_f32_16x16x32_bf16 %0, %1, %2, 0" : "=v"(c) : "v"(a), "v"(b));
  else asm volatile("v_mfma_f32_16x16x32_bf16 %0, %1, %2, %0" : "+v"(c) : "v"(a), "v"(b));
}
DI void prio_up(unsigned cond) { asm volatile("s_cmp_eq_u32 %0, 0\n\ts_cbranch_scc1 1f\n\ts_setprio 1\n1:" :: "s"(cond) : "scc"); }
DI s16x4 trd(lptr p) { return __builtin_amdgcn_ds_read_tr16_b64_v4i16((LAS s16x4*)p); }
DI bf16x8 trfrag(lptr p0, lptr p1) { s16x4 lo = trd(p0), hi = trd(p1); return __builtin_shufflevector(lo, hi, 0, 1, 2, 3, 4, 5, 6, 7); }
DI bf16x8 ldfrag(lptr p) { return *(LAS bf16x8*)p; }
DI bf16x8 asfrag(u32x4 w) { return __builtin_bit_cast(bf16x8, w); }
DI float swap_max(float m) { auto rr = __builtin_amdgcn_permlane32_swap(__float_as_uint(m), __float_as_uint(m), false, false);
  return __builtin_fmaxf(__uint_as_float(rr[0]), __uint_as_float(rr[1])); }
DI float swap_add(float m) { auto rr = __builtin_amdgcn_permlane32_swap(__float_as_uint(m), __float_as_uint(m), false, false);
  return __uint_as_float(rr[0]) + __uint_as_float(rr[1]); }
DI float swap16_add(float a, float b) { auto rr = __builtin_amdgcn_permlane16_swap(__float_as_uint(a), __float_as_uint(b), false, false);
  return __uint_as_float(rr[0]) + __uint_as_float(rr[1]); }
DI float swap32_add(float a, float b) { auto rr = __builtin_amdgcn_permlane32_swap(__float_as_uint(a), __float_as_uint(b), false, false);
  return __uint_as_float(rr[0]) + __uint_as_float(rr[1]); }
DI float ror8(float a) { return __int_as_float(__builtin_amdgcn_update_dpp(0, __float_as_int(a), 0x128, 0xf, 0xf, false)); }
DI void lbar() { asm volatile("s_waitcnt lgkmcnt(0)\n\ts_barrier" ::: "memory"); }
DI void signal(rsrc_t r, unsigned soff, u32x2 tag, unsigned prog, int tid) { if (tid == 0) __builtin_amdgcn_raw_buffer_store_b64(u32x2{tag[0] | prog, tag[1]}, r, 0, soff, 16); }
DI void gld8c(u32x2& v, unsigned voff, rsrc_t r, unsigned soff) { asm volatile("buffer_load_dwordx2 %0, %1, %2, %3 offen sc1" : "=v"(v) : "v"(voff), "s"(r), "s"(soff) : "memory"); }
DI bool all_lanes(bool c) { return __builtin_amdgcn_ballot_w64(c) == ~0ull; }
DI bool flag_ok(u32x2 v, u32x2 tag, unsigned need) { return v[1] == tag[1] && v[0] - (tag[0] + need) <= 15u - need; }
DI void poll_until(u32x2 v, unsigned voff, rsrc_t r, unsigned soff, u32x2 tag, unsigned need, unsigned own) {
  for (int sp = 0; !all_lanes(flag_ok(v, tag, need) || voff == own) && sp < (1 << 20); ++sp) {
    __builtin_amdgcn_s_sleep(2);
    asm volatile("buffer_load_dwordx2 %0, %1, %2, %3 offen sc1\n\ts_waitcnt vmcnt(0)" : "=v"(v) : "v"(voff), "s"(r), "s"(soff) : "memory");
  }
}
DI bf16x8 join16(unsigned a0, unsigned a1, unsigned b0, unsigned b1) {
  auto r0 = __builtin_amdgcn_permlane16_swap(a0, b0, false, false); auto r1 = __builtin_amdgcn_permlane16_swap(a1, b1, false, false);
  return __builtin_bit_cast(bf16x8, u32x4{r0[0], r1[0], r0[1], r1[1]});
}
DI u32x4 pack8(const f32x16& a, int s) {
  u32x4 w = {cvtpk(a[8 * s], a[8 * s + 1]), cvtpk(a[8 * s + 2], a[8 * s + 3]), cvtpk(a[8 * s + 4], a[8 * s + 5]), cvtpk(a[8 * s + 6], a[8 * s + 7])};
  return w;
}
DI lptr lp(unsigned a) { return (lptr)(uintptr_t)a; }
DI void bump(unsigned& a, int d) { a += d; PIN(a); }
template <int N> DI void wait_bar() { asm volatile("s_waitcnt vmcnt(%0) lgkmcnt(0)\n\ts_barrier" :: "i"(N) : "memory"); }
template <int I, int N, class F> DI void sfor(F&& f) { if constexpr (I < N) { f(std::integral_constant<int, I>{}); sfor<I + 1, N>(f); } }

#define STAMP(i)

constexpr int A_SLOT = 32768, A_SPARE = 4 * A_SLOT, A_LDS = 5 * A_SLOT, A_JUNK = A_SPARE + 16384;
constexpr float L_LIMIT = 1.2379400392853803e27f;
constexpr float M_MARGIN = 40.f;

#define XNA 3
#define GU32 0
struct AttnState {
  f32x16 O[4], S[2], negm; u32x4 Pk[2][2]; bf16x8 Q[8], F[8]; float l;
};

constexpr int B_SLOT = 36864, B_SPARE = 4 * B_SLOT, SP_X = B_SPARE + 5120, SP_RED = B_SPARE + 14336;
constexpr int bslot(int C) { return ((C + 2) & 3) * B_SLOT; }

__global__ __launch_bounds__(512, 2) void kmain(const u16* __restrict__ x, const u16* __restrict__ wq, const u16* __restrict__ kc,
                                                const u16* __restrict__ vc, const u16* __restrict__ wo, const u16* __restrict__ wg,
                                                const u16* __restrict__ wu, const u16* __restrict__ wd, const u16* __restrict__ cvec,
                                                const u16* __restrict__ wmod, char* __restrict__ ws, u16* __restrict__ out) {
  const u16* xh = (const u16*)(ws + WS_XH);
  extern __shared__ __attribute__((aligned(16))) char smem[];
  const lptr L = (lptr)smem;
  const unsigned lds0 = (unsigned)(uintptr_t)L;
  const int tid = threadIdx.x, lane = tid & 63;
  const unsigned w = rfl(tid >> 6), g = w & 3, hd = w >> 2;
  const unsigned j = 2 * (blockIdx.x & 7) + ((blockIdx.x >> 3) & 1), rb = blockIdx.x >> 4, rg = rb * 4 + g;
  const int r32 = lane & 31, h = lane >> 5, r8 = (lane >> 2) & 7, cp = lane & 3, blk = (lane >> 4) & 1, q4 = (lane >> 2) & 3;
  STAMP(0);
  const rsrc_t rWq = mkrsrc(wq, D_ * 4096 * 2), rK = mkrsrc(kc, HKV * SK * DH * 2), rV = mkrsrc(vc, HKV * SK * DH * 2),
               rWo = mkrsrc(wo, 4096 * D_ * 2), rX = mkrsrc(xh, S_ * D_ * 2);
  const unsigned voffWq = (16 * (w >> 1) + 8 * (r8 >> 2) + 4 * (w & 1) + (r8 & 3)) * 8192 + h * 64 + ((cp ^ ((r8 >> 2) * 2)) * 16) + 512 * j;
  const unsigned voffX = lane * 16 + rg * (NKS * 1024);
  const unsigned kvrow = 8 * (w >> 1) + r8;
  const unsigned voffV = kvrow * 256 + (2 * (w & 1) + h) * 64 + cp * 16;
  const unsigned voffK = kvrow * 256 + (2 * (w & 1) + h) * 64 + ((cp ^ ((kvrow >> 2) & 3)) * 16);
  const unsigned kvbase = (j * SK + (SK - WIN)) * 256;
  const unsigned voffWo = (256 * j + 32 * w + r8) * 1152 + h * 64 + cp * 16;
  const unsigned m0w4 = lds0 + w * 4096, m0w1 = lds0 + w * 1024;

  auto issue = [&](auto Cc, auto Pc) AI {
    constexpr int C = decltype(Cc)::value, P = decltype(Pc)::value, slot = (C & 3) * A_SLOT;
    if constexpr (C < 9) dma16<128 * P>(m0w4 + slot + P * 896, voffWq, rWq, C * 64 * 8192);
    else if constexpr (C < 17) {
      constexpr int i = C - 9;
      constexpr bool isK = (P < 2 && !(i == 7 && P == 1)) || (i == 0 && P == 2);
      constexpr int u = (i == 0 && P == 2) ? 0 : (i == 7 && P == 1) ? 15 : (P < 2 ? 2 * i + 1 + P : 2 * i - 3 + P);
      if constexpr (isK) dma16<0>(m0w1 + slot + P * 8192, voffK, rK, kvbase + u * 8192);
      else               dma16<0>(m0w1 + slot + P * 8192, voffV, rV, kvbase + u * 8192);
    } else if constexpr (C < 26) dma16<0>(m0w4 + slot + P * 1024, voffWo, rWo, (C - 17) * 128 + P * 8 * 1152);
  };
  u32x4 X[NKS];
  auto issueX = [&](auto Cc, auto Pc) AI {
    constexpr int C = decltype(Cc)::value, P = decltype(Pc)::value;
    if constexpr (C < 9) gld16c<1024 * P>(X[4 * C + P], voffX, rX, C * 4096);
  };
  const rsrc_t rC = mkrsrc(ws, 65536), rXr = mkrsrc(x, S_ * D_ * 2);
  const unsigned voffF = (lane & 15) * 8;
  unsigned long long pc0; unsigned clen;
  asm volatile("s_getpc_b64 %0\n.Lkm_begin:\n\ts_mov_b32 %1, .Lkm_end-.Lkm_begin" : "=s"(pc0), "=s"(clen));
  const rsrc_t rCode = mkrsrc((const void*)pc0, clen);
  if (w == 7) touch(lds0 + A_JUNK, ((blockIdx.x >> 3) * 32 + (lane & 31)) * 128, rCode);
  u32x2 tag;
  {
    const int e = lane >> 3, row8 = lane & 7;
    u32x2 ev; u32x4 wm[3], xa, xb; unsigned cv[3];
    asm volatile("buffer_load_dwordx2 %0, off, %1, %2 sc1" : "=v"(ev) : "s"(rC), "s"(WS_EPOCH) : "memory");
    dma16<0>(m0w1 + A_SPARE + 4096, e * 1152 + row8 * 16 + w * 128, rXr, (rb * 128 + j * 8) * 1152);
    gld16<1024>(xb, row8 * 1152 + w * 16, rXr, (rb * 128 + j * 8) * 1152);
    const rsrc_t rM = mkrsrc(wmod, D_ * 3456 * 2), rCv = mkrsrc(cvec, D_ * 2);
    const unsigned pb = ((blockIdx.x & 7) + 8 * (blockIdx.x >> 5)) * 4 + ((blockIdx.x >> 3) & 3);
    const unsigned vm = (tid >> 1) * 6912 + (tid & 1) * 16 + pb * 32;
#pragma unroll
    for (int i = 0; i < 3; ++i) {
      gld16<0>(wm[i], vm, rM, i * 256 * 6912);
      asm volatile("buffer_load_ushort %0, %1, %2, %3 offen" : "=v"(cv[i]) : "v"(tid & ~1), "s"(rCv), "s"(i * 512) : "memory");
    }
    asm volatile("s_waitcnt vmcnt(0)" ::: "memory");
    if (w >= 6) {
      const unsigned u = tid - 384;
      touch(lds0 + A_JUNK, (36 * rb + (u >> 2)) * 8192 + 512 * j + (u & 3) * 128, rWq);
      if (w == 6) touch(lds0 + A_JUNK, (36 * rb + 32 + ((lane & 15) >> 2)) * 8192 + 512 * j + (u & 3) * 128, rWq);
    }
    asm volatile("" : "+v"(ev), "+v"(wm[0]), "+v"(wm[1]), "+v"(wm[2]), "+v"(cv[0]), "+v"(cv[1]), "+v"(cv[2]), "+v"(xb) :: "memory");
    xa = *(LAS u32x4*)(L + A_SPARE + 4096 + w * 1024 + row8 * 128 + e * 16);
    STAMP(16);
    { const unsigned long long t = (((unsigned long long)rfl(ev[1]) << 32) | rfl(ev[0])) + 16ull; tag[0] = (unsigned)t & ~15u; tag[1] = (unsigned)(t >> 32); }
    const unsigned gl = tag[0] << 12, gh = (tag[1] << 12) | (tag[0] >> 20);
    float a[8];
#pragma unroll
    for (int q = 0; q < 8; ++q) a[q] = 0.f;
#pragma unroll
    for (int i = 0; i < 3; ++i) {
      const float c0 = bflo(cv[i]), sc = bfr(c0 * __builtin_amdgcn_rcpf(1.f + __builtin_amdgcn_exp2f(c0 * -1.4426950408889634f)));
#pragma unroll
      for (int q = 0; q < 4; ++q) { a[2 * q] += sc * bflo(wm[i][q]); a[2 * q + 1] += sc * bfhi(wm[i][q]); }
    }
    const bool b3 = e & 1;
    const float a0 = swap32_add(a[0], a[4]), a1 = swap32_add(a[1], a[5]), a2 = swap32_add(a[2], a[6]), a3 = swap32_add(a[3], a[7]);
    const float b0 = swap16_add(a0, a2), b1 = swap16_add(a1, a3);
    float ms = (b3 ? b1 : b0) + ror8(b3 ? b0 : b1);
    ms += __shfl_xor(ms, 2); ms += __shfl_xor(ms, 4);
    const lptr red = L + A_SPARE;
    if ((lane & 6) == 0) *(LAS float*)(red + ((lane & 1) * 8 + e) * 32 + w * 4) = ms;
    float v[16];
#pragma unroll
    for (int q = 0; q < 4; ++q) { v[2 * q] = bflo(xa[q]); v[2 * q + 1] = bfhi(xa[q]); v[8 + 2 * q] = bflo(xb[q]); v[9 + 2 * q] = bfhi(xb[q]); }
    float s0 = 0.f, s1 = 0.f;
#pragma unroll
    for (int q = 0; q < 8; ++q) { s0 += v[q]; s1 += v[8 + q]; }
    s0 += e == 0 ? s1 : 0.f;
    s0 += ror8(s0); s0 = swap16_add(s0, s0); s0 = swap32_add(s0, s0);
    if (lane < 8) *(LAS float*)(red + 512 + lane * 32 + w * 4) = s0;
    lbar();
    auto sum8 = [&](lptr q) AI { const f32x4 r0 = *(LAS f32x4*)q, r1 = *(LAS f32x4*)(q + 16);
      return ((r0[0] + r0[1]) + (r0[2] + r0[3])) + ((r1[0] + r1[1]) + (r1[2] + r1[3])); };
    if (tid < 16 && pb < 216) {
      float m = bfr(sum8(red + tid * 32));
      const unsigned vec = pb / 36;
      if (vec == 1 || vec == 4) m = bfr(1.f + m);
      if (vec >= 2) {
        __builtin_amdgcn_raw_buffer_store_b16((u16)(__float_as_uint(m) >> 16), rC, tid * 2, WS_TABB + (pb - 72) * 32, 16);
        asm volatile("s_waitcnt vmcnt(0)" ::: "memory");
      }
      const u32x2 gr = {gl | (__float_as_uint(m) >> 16), gh};
      __builtin_amdgcn_raw_buffer_store_b64(gr, rC, tid * 8, WS_GRAN + pb * 128, 16);
    }
    STAMP(17);
    const float mean = sum8(red + 512 + row8 * 32) * (1.f / D_);
    s0 = 0.f; s1 = 0.f;
#pragma unroll
    for (int q = 0; q < 8; ++q) { v[q] -= mean; v[8 + q] -= mean; s0 += v[q] * v[q]; s1 += v[8 + q] * v[8 + q]; }
    s0 += e == 0 ? s1 : 0.f;
    s0 += ror8(s0); s0 = swap16_add(s0, s0); s0 = swap32_add(s0, s0);
    if (lane < 8) *(LAS float*)(red + 768 + lane * 32 + w * 4) = s0;
#define MIDC 2
    sfor<0, MIDC>([&](auto C) AI { sfor<0, 4>([&](auto P) AI { issue(C, P); }); });
    u32x2 g0, g1, g2;
    const lptr tab = red + 1024;
    if (w < 6) {
      for (int sp = 0; sp < (1 << 20); ++sp) {
        asm volatile("buffer_load_dwordx2 %0, %3, %4, %5 offen sc1\n\tbuffer_load_dwordx2 %1, %3, %4, %5 offen offset:3072 sc1\n\tbuffer_load_dwordx2 %2, %3, %4, %6 offen sc1\n\ts_waitcnt vmcnt(0)"
                     : "=&v"(g0), "=&v"(g1), "=&v"(g2) : "v"(tid * 8), "s"(rC), "s"(WS_GRAN), "s"(WS_GRAN + 6144) : "memory");
        if (all_lanes((((g0[0] ^ gl) | (g1[0] ^ gl) | (g2[0] ^ gl)) >> 16 | (g0[1] ^ gh) | (g1[1] ^ gh) | (g2[1] ^ gh)) == 0u)) break;
        __builtin_amdgcn_s_sleep(1);
      }
      *(LAS u16*)(tab + tid * 2) = (u16)g0[0]; *(LAS u16*)(tab + 768 + tid * 2) = (u16)g1[0]; *(LAS u16*)(tab + 1536 + tid * 2) = (u16)g2[0];
    }
    lbar();
    STAMP(15);
    const float rs = rsqrtf(sum8(red + 768 + row8 * 32) * (1.f / D_) + LN_EPS);
    const u32x4 sa = *(LAS u32x4*)(tab + w * 128 + e * 16), ca = *(LAS u32x4*)(tab + 1152 + w * 128 + e * 16);
    const u32x4 sb = *(LAS u32x4*)(tab + 1024 + w * 16), cb = *(LAS u32x4*)(tab + 1152 + 1024 + w * 16);
    u32x4 ha, hb;
#pragma unroll
    for (int q = 0; q < 4; ++q) {
      ha[q] = cvtpk((v[2 * q] * rs * bflo(ca[q]) + bflo(sa[q])) * QSCALE, (v[2 * q + 1] * rs * bfhi(ca[q]) + bfhi(sa[q])) * QSCALE);
      hb[q] = cvtpk((v[8 + 2 * q] * rs * bflo(cb[q]) + bflo(sb[q])) * QSCALE, (v[9 + 2 * q] * rs * bfhi(cb[q]) + bfhi(sb[q])) * QSCALE);
    }
    const unsigned ho = (rb * 4 + (j >> 2)) * 36864 + ((j >> 1) & 1) * 1024 + ((j & 1) * 8 + row8) * 16;
    __builtin_amdgcn_raw_buffer_store_b128(ha, rX, ho + (2 * w + (e >> 2)) * 2048 + (e & 3) * 256, 0, 16);
    if (lane < 8) __builtin_amdgcn_raw_buffer_store_b128(hb, rX, ho + (16 + (w >> 2)) * 2048 + (w & 3) * 256, 0, 16);
    asm volatile("s_waitcnt vmcnt(0)\n\ts_barrier" ::: "memory");
    STAMP(18);
    signal(rC, 7 * 2048 + rb * 128 + j * 8, tag, 1, tid);
    sfor<MIDC, 4>([&](auto C) AI { sfor<0, 4>([&](auto P) AI { issue(C, P); }); });
    const unsigned vg = (tid < 144 ? tid : 0) * 128;
    for (int sp = 0; sp < (1 << 20); ++sp) {
      asm volatile("buffer_load_dwordx2 %0, %2, %4, %5 offen sc1\n\tbuffer_load_dwordx2 %1, %3, %4, %6 offen sc1\n\ts_waitcnt vmcnt(0)"
                   : "=&v"(g0), "=&v"(g1) : "v"(voffF), "v"(vg), "s"(rC), "s"(7 * 2048 + rb * 128), "s"(WS_GRAN + 1152 * 8) : "memory");
      if (all_lanes((flag_ok(g0, tag, 1) || voffF == j * 8) && ((g1[0] ^ gl) >> 16 | (g1[1] ^ gh)) == 0u)) break;
      __builtin_amdgcn_s_sleep(1);
    }
    STAMP(19);
  }
  sfor<0, 3>([&](auto C) AI { sfor<0, 4>([&](auto P) AI { issueX(C, P); }); });

  const int trb = h * 256 + q4 * 64 + blk * 32;
  unsigned aWq = lds0 + h * 8192 + (blk * 4 + q4) * 64 + blk * 32 + cp * 8 + hd * 2048, aWq1 = aWq ^ 32;
  PIN(aWq); PIN(aWq1);
  AttnState st;
  {
    f32x4 acc[8][2];
    auto stage = [&](auto Nc) AI {
      constexpr int n = decltype(Nc)::value, T = n & 7, C = n >> 4, ksl = (n >> 3) & 1;
      if constexpr ((n & 15) == 0) {
        if constexpr (C > 0) { bump(aWq, ((C & 3) - ((C - 1) & 3)) * A_SLOT); bump(aWq1, ((C & 3) - ((C - 1) & 3)) * A_SLOT); }
        constexpr int NV = C < 2 ? 8 : C == 2 ? 12 : (C + 1 < 9 ? 8 : 4) + (C + 2 < 9 ? 8 : 4);
        asm volatile("s_waitcnt vmcnt(%4) lgkmcnt(0)\n\ts_barrier" : "+v"(X[4 * C]), "+v"(X[4 * C + 1]), "+v"(X[4 * C + 2]), "+v"(X[4 * C + 3]) : "i"(NV) : "memory");
      }
      const lptr b = lp((T & 1) ? aWq1 : aWq) + (4 * ksl * 8 + (T >> 1)) * 512;
      st.F[n & 7] = trfrag(b, b + 4096);
      if constexpr (n & 1) {
        if constexpr (n & 2) { if constexpr (C > 0) issue(std::integral_constant<int, C + 3>{}, std::integral_constant<int, ((n & 15) >> 2)>{}); }
        else issueX(std::integral_constant<int, C + 3>{}, std::integral_constant<int, ((n & 15) >> 2)>{});
      }
    };
    sfor<0, 4>([&](auto N) AI { stage(N); SBAR(); });
    STAMP(1);
    sfor<0, 144>([&](auto Mc) AI {
      constexpr int m = decltype(Mc)::value;
      mfma16<(m < 8)>(acc[m & 7][0], st.F[m & 7], X[2 * (m >> 3)]);
      if constexpr (m + 4 < 144) stage(std::integral_constant<int, m + 4>{});
      mfma16<(m < 8)>(acc[m & 7][1], st.F[m & 7], X[2 * (m >> 3) + 1]);
      SBAR();
    });
    asm volatile("s_nop 7"); SBAR();
#pragma unroll
    for (int t = 0; t < 8; ++t)
      st.Q[t] = join16(cvtpk(acc[t][0][0], acc[t][0][1]), cvtpk(acc[t][0][2], acc[t][0][3]), cvtpk(acc[t][1][0], acc[t][1][1]), cvtpk(acc[t][1][2], acc[t][1][3]));
  }
  STAMP(2);
  unsigned aK0 = lds0 + (9 & 3) * A_SLOT + 2048 * (r32 >> 3) + 64 * (r32 & 7) + 16 * ((0 + h) ^ ((r32 >> 2) & 3));
  unsigned aK1 = aK0 ^ 32, aV = lds0 + (9 & 3) * A_SLOT + trb + cp * 8;
  PIN(aK0); PIN(aK1); PIN(aV);
#pragma unroll
  for (int t = 0; t < 4; ++t) st.O[t] = f32x16{};
  auto kfrag = [&](int off, auto Ic) AI { constexpr int i = decltype(Ic)::value; return ldfrag(lp((i & 1) ? aK1 : aK0) + off + 512 * (i >> 1)); };
  auto vfrag = [&](int off, auto Ic) AI { constexpr int i = decltype(Ic)::value, ks = i >> 2, T = i & 3;
    return trfrag(lp(aV) + off + ((2 * ks) * 4 + T) * 512, lp(aV) + off + ((2 * ks + 1) * 4 + T) * 512); };
  auto step = [&](auto Par, auto HQK, auto HPV, auto NXT, auto NVc, int u, int ko, int vo, int kn, int dsl, auto&& dmaf) AI {
    constexpr int par = decltype(Par)::value, nxt = decltype(NXT)::value, NV = decltype(NVc)::value;
    constexpr bool hqk = decltype(HQK)::value, hpv = decltype(HPV)::value;
    float l = st.l, eprev = 0.f;
    sfor<0, 16>([&](auto Mc) AI {
      constexpr int m = decltype(Mc)::value, n = m + 4;
      if constexpr (n < 8) { if constexpr (hqk) st.F[n & 7] = kfrag(ko, std::integral_constant<int, n>{}); }
      else if constexpr (n < 16) { if constexpr (hpv) st.F[n & 7] = vfrag(vo, std::integral_constant<int, n - 8>{}); }
      else {
        if constexpr (n == 16 && NV >= 0) { bump(aK0, dsl); bump(aK1, dsl); bump(aV, dsl); wait_bar<NV>(); }
        if constexpr (nxt == 1) st.F[n & 7] = kfrag(kn, std::integral_constant<int, n - 16>{});
      }
      dmaf(Mc);
      if constexpr (m < 8) { if constexpr (hqk) st.S[par ^ 1] = MFMA(st.F[m & 7], st.Q[m], m == 0 ? st.negm : st.S[par ^ 1]); }
      else { if constexpr (hpv) st.O[m & 3] = MFMA(st.F[m & 7], asfrag(st.Pk[par ^ 1][(m - 8) >> 2]), st.O[m & 3]); }
      const float e = __builtin_amdgcn_exp2f(st.S[par][m]);
      l += e; PIN(l);
      if constexpr (m & 1) { st.Pk[par][m >> 3][(m >> 1) & 3] = cvtpk(eprev, e); PIN(st.Pk[par][m >> 3]); }
      else { eprev = e; PIN(eprev); }
      SBAR();
    });
    st.l = l;
  };
  using I0 = std::integral_constant<int, 0>; using I1 = std::integral_constant<int, 1>; using IM = std::integral_constant<int, -1>;
  using BT = std::true_type; using BF = std::false_type;
  {
    wait_bar<8>();
    sfor<0, 4>([&](auto P) AI { issue(std::integral_constant<int, 12>{}, P); });
    f32x16 s = f32x16{};
    sfor<0, 8>([&](auto I) AI { s = MFMA(kfrag(2 * 8192, I), st.Q[decltype(I)::value], s); });
    sfor<0, 4>([&](auto I) AI { st.F[decltype(I)::value] = kfrag(0, I); });
    float mt = s[0];
#pragma unroll
    for (int r = 1; r < 16; ++r) mt = __builtin_fmaxf(mt, s[r]);
    mt = swap_max(mt) + M_MARGIN;
#pragma unroll
    for (int r = 0; r < 16; ++r) { st.S[0][r] = s[r] - mt; st.negm[r] = -mt; }
    PIN(st.negm);
    st.l = 0.f;
    SBAR();
  }
  auto nodma = [&](auto) AI {};
  auto warmF = [&](auto Uc) AI {
    constexpr int u = decltype(Uc)::value;
    if constexpr (u < 12) { if (w == (u & 7)) {
        unsigned t = lane; PIN(t); t += (u % 3) * 64;
        if constexpr (u < 3) touch(lds0 + A_JUNK, (256 * j + 16 * rb) * 1152 + (t < 144 ? t : 0) * 128, rWo);
        else if constexpr (u < 9) { const unsigned rr = t / 5, cc = t - rr * 5;
          touch(lds0 + A_JUNK, (36 * rb + (rr < 36 ? rr : 0)) * (F_ * 2) + ((576 * j) & ~127u) + cc * 128, mkrsrc(u < 6 ? wg : wu, D_ * F_ * 2)); }
        else touch(lds0 + A_JUNK, (288 * j + 18 * rb) * 1152 + (t < 162 ? t : 0) * 128, mkrsrc(wd, F_ * D_ * 2));
    } }
  };
  sfor<0, 8>([&](auto Ic) AI {
    constexpr int i = decltype(Ic)::value, C = 9 + i, dsl = (((C + 1) & 3) - (C & 3)) * A_SLOT;
    auto dmaf = [&](auto Mc) AI { constexpr int m = decltype(Mc)::value;
      if constexpr (i > 0 && (m & 3) == 1) issue(std::integral_constant<int, C + 3>{}, std::integral_constant<int, (m >> 2)>{});
      if constexpr (m == 15) warmF(std::integral_constant<int, 2 * i>{}); };
    if constexpr (i == 0) step(I0{}, BT{}, BF{}, I1{}, IM{}, 0, 0, 0, 8192, 0, dmaf);
    else if constexpr (i < 7) step(I0{}, BT{}, BT{}, I1{}, IM{}, 2 * i, 0, 2 * 8192, 8192, 0, dmaf);
    else step(I0{}, BT{}, BT{}, I0{}, IM{}, 2 * i, 0, 2 * 8192, 0, 0, dmaf);
    auto warm = [&](auto Mc) AI { if constexpr (decltype(Mc)::value == 6) warmF(std::integral_constant<int, 2 * i + 1>{}); };
    if constexpr (i < 7) step(I1{}, BT{}, BT{}, I1{}, std::integral_constant<int, 8>{}, 2 * i + 1, 8192, 3 * 8192, 0, dsl, warm);
    else step(I1{}, BF{}, BT{}, I0{}, IM{}, 15, 0, 3 * 8192, 0, 0, nodma);
  });
  bf16x8 Of[16];
  {
    constexpr int s16 = (16 & 3) * A_SLOT;
    sfor<0, 8>([&](auto Ic) AI { constexpr int i = decltype(Ic)::value; st.O[i & 3] = MFMA(vfrag(8192, Ic), asfrag(st.Pk[1][i >> 2]), st.O[i & 3]); });
    STAMP(3);
    wait_bar<8>();
    if (__builtin_expect(__builtin_amdgcn_ballot_w64(!(st.l <= L_LIMIT)) != 0, 0)) {
      const u16* kg = kc + ((size_t)j * SK + (SK - WIN) + r32) * DH + 8 * h;
      const u16* vg = vc + ((size_t)j * SK + (SK - WIN) + 4 * h) * DH + r32;
      float mt = -__builtin_inff();
#pragma unroll 1
      for (int u = 0; u < 16; ++u) {
        f32x16 s = f32x16{};
#pragma unroll
        for (int i = 0; i < 8; ++i) s = MFMA(*(const bf16x8*)(kg + (size_t)u * 32 * DH + 16 * i), st.Q[i], s);
#pragma unroll
        for (int r = 0; r < 16; ++r) mt = __builtin_fmaxf(mt, s[r]);
      }
      mt = swap_max(mt);
      float l = 0.f;
#pragma unroll
      for (int t = 0; t < 4; ++t) st.O[t] = f32x16{};
#pragma unroll 1
      for (int u = 0; u < 16; ++u) {
        f32x16 s = f32x16{};
#pragma unroll
        for (int i = 0; i < 8; ++i) s = MFMA(*(const bf16x8*)(kg + (size_t)u * 32 * DH + 16 * i), st.Q[i], s);
#pragma unroll
        for (int r = 0; r < 16; ++r) { s[r] = __builtin_amdgcn_exp2f(s[r] - mt); l += s[r]; }
#pragma unroll
        for (int ks = 0; ks < 2; ++ks) {
          const bf16x8 pf = asfrag(pack8(s, ks));
#pragma unroll
          for (int t = 0; t < 4; ++t) {
            bf16x8 vf;
#pragma unroll
            for (int e = 0; e < 8; ++e) vf[e] = (short)vg[(size_t)(u * 32 + 16 * ks + 8 * (e >> 2) + (e & 3)) * DH + 32 * t];
            st.O[t] = MFMA(vf, pf, st.O[t]);
          }
        }
      }
      st.l = l;
    }
    const float inv = __builtin_amdgcn_rcpf(swap_add(st.l));
#pragma unroll
    for (int t = 0; t < 4; ++t) {
#pragma unroll
      for (int r = 0; r < 16; ++r) st.O[t][r] *= inv;
      Of[2 * t] = asfrag(pack8(st.O[t], 0)); Of[2 * t + 1] = asfrag(pack8(st.O[t], 1));
    }
    const lptr xw = L + (hd ? s16 : A_SPARE) + g * 8192 + lane * 16, xr = L + (hd ? A_SPARE : s16) + g * 8192 + lane * 16;
#pragma unroll
    for (int i = 0; i < 8; ++i) *(LAS bf16x8*)(xw + i * 1024) = Of[i];
    wait_bar<8>();
#pragma unroll
    for (int i = 0; i < 8; ++i) Of[8 + i] = ldfrag(xr + i * 1024);
    asm volatile("s_waitcnt lgkmcnt(0)\n\ts_barrier" ::: "memory");
    SBAR();
  }
  STAMP(4);
  const unsigned hf = hd;
  const rsrc_t rGU = mkrsrc(hf ? wu : wg, D_ * F_ * 2), rWd = mkrsrc(wd, F_ * D_ * 2), rH2 = mkrsrc(ws + WS_H2, S_ * D_ * 2);
  unsigned lane2 = lane; PIN(lane2);
  unsigned voffGU[5];
  auto setGU = [&](auto NSc, auto Pc, unsigned ln) AI {
    constexpr int NS = decltype(NSc)::value, p = decltype(Pc)::value;
    const unsigned r8v = (ln >> 2) & 7;
    const unsigned piece = p < 4 ? g + 4 * p : 16 + (g & 1), stl = 2 * piece + (ln >> 5), R = stl / NS, sub = stl - NS * R;
    voffGU[p] = (8 * R + r8v) * (F_ * 2) + 576 * j + sub * 64 + (((ln & 3) ^ (GU32 ? hf * 2 : (r8v >> 2) * 2)) * 16);
    PIN(voffGU[p]);
  };
  sfor<0, 5>([&](auto P) AI { setGU(std::integral_constant<int, 9>{}, P, lane2); });
  const unsigned m0gu = lds0 + hf * 18432 + g * 1024, m0gux = lds0 + hf * 18432 + (16 + (g & 1)) * 1024;
  const bool xgu = (g >> 1) != 0;
  unsigned voffWd = 0;
  auto issueF = [&](auto Cc, auto Pc) AI {
    constexpr int C = decltype(Cc)::value, P = decltype(Pc)::value, slot = bslot(C);
    if constexpr (C < 18) {
      constexpr unsigned so = C < 12 ? C * 32 * F_ * 2 : (384 + 96 * (C & 1)) * F_ * 2 + 192 * ((C - 12) >> 1);
      if constexpr (P < 4) dma16<0>(m0gu + slot + P * 4096, voffGU[P], rGU, so);
      else { if (xgu == (bool)(C & 1)) dma16<0>(m0gux + slot, voffGU[4], rGU, so); }
    } else if constexpr (C < 27) {
      if constexpr (P < 4) dma16<0>(m0w1 + slot + P * 8192, voffWd, rWd, (C - 18) * 128 + P * 64 * 1152);
      else { if (xgu == (bool)(C & 1)) {
          const unsigned e = (g & 1) + 2 * hf;
          dma16<0>(lds0 + (32 + e) * 1024 + slot, voffWd, rWd, (C - 18) * 128 + (GU32 ? (32 + e - w) * 8 : 256 + 16 * hf + 4 * (g & 1) - 16 * (w >> 1) - 4 * (w & 1)) * 1152); } }
    }
  };
  const rsrc_t rP = mkrsrc(ws + WS_P, 16 * ACT_B);
  auto project = [&](auto NKc, auto CBc, const bf16x8* Bf, unsigned aOwn, unsigned aPar, auto&& dslot, auto&& nv, auto&& dma, auto&& preload, auto NXc, auto&& extra, float (&y)[9]) AI {
    constexpr int NK = decltype(NKc)::value, HK = NK / 2, CB = decltype(CBc)::value, NX = decltype(NXc)::value, NA = XNA;
    unsigned ln = lane; PIN(ln);
    const unsigned cons = 4 * g + ((ln & 31) >> 3);
    const unsigned voffP = rb * 16 * 147456 + (CB == 0 ? cons * 147456 + j * 128 : j * 147456 + cons * 128) + (4 * hd + (ln >> 5)) * 2048 + (ln & 7) * 16;
    const unsigned vo = rb * 16 * 147456 + w * 2048 + (CB == 0 ? j * 147456 + ln * 16 : (ln >> 3) * 147456 + j * 128 + (ln & 7) * 16);
    const unsigned co = rb * 128 + CB * 2048;
    PIN(aOwn); PIN(aPar);
    bf16x8 F[8]; f32x16 acc[2]; u32x4 pv[18]; u32x2 pl;
    const unsigned vf = (ln & 15) * 8;
    auto ldp = [&](auto Tc) AI { constexpr int t = decltype(Tc)::value; gld16c<0>(pv[t], vo, rP, (t >> 1) * 16384 + (t & 1) * (CB == 0 ? 1024 : 8 * 147456)); };
    auto red = [&](auto Tc) AI {
      constexpr int t = decltype(Tc)::value;
      const bool b3 = (ln >> 3) & 1;
      float sm[8];
#pragma unroll
      for (int e = 0; e < 4; ++e) { sm[2 * e] = bflo(pv[2 * t][e]) + bflo(pv[2 * t + 1][e]); sm[2 * e + 1] = bfhi(pv[2 * t][e]) + bfhi(pv[2 * t + 1][e]); }
      const float a0 = swap32_add(sm[0], sm[4]), a1 = swap32_add(sm[1], sm[5]), a2 = swap32_add(sm[2], sm[6]), a3 = swap32_add(sm[3], sm[7]);
      const float b0 = swap16_add(a0, a2), b1 = swap16_add(a1, a3);
      y[t] = (b3 ? b1 : b0) + ror8(b3 ? b0 : b1);
    };
    auto stage = [&](auto Nc) AI {
      constexpr int n = decltype(Nc)::value, i = n / NK, ks = n % NK;
      if constexpr (ks == 0 && i > 0) {
        bump(aOwn, dslot(i)); bump(aPar, dslot(i));
        constexpr int NV = decltype(nv(std::integral_constant<int, i>{}))::value;
        if constexpr (NA > 0 && i == NA + 5) {
          asm volatile("s_waitcnt vmcnt(%1)" : "+v"(pl) : "i"(NV) : "memory");
          poll_until(pl, vf, rC, co, tag, NA, j * 8);
          lbar();
        } else wait_bar<NV>();
        if constexpr ((NA > 0 && i == NA + 2) || i == 8) signal(rC, co + j * 8, tag, i - 2, tid);
        if constexpr (NA > 0 && i == NA + 4) gld8c(pl, vf, rC, co);
      }
      const lptr b = lp(ks < HK ? aOwn : aPar) + (CB == 0 ? ks % HK : 6 * ((ks % HK) / 3) + ks % 3) * 2048;
      F[n & 7] = trfrag(b, b + 1024);
      if constexpr (NA > 0 && i == NA + 5 && ks < 2 * NA) ldp(std::integral_constant<int, ks>{});
      dma(std::integral_constant<int, i>{}, std::integral_constant<int, ks>{});
    };
    sfor<0, 4>([&](auto N) AI { stage(N); SBAR(); });
    sfor<0, 9 * NK>([&](auto Mc) AI {
      constexpr int m = decltype(Mc)::value, i = m / NK, ks = m % NK;
      if constexpr (m + 4 < 9 * NK) stage(std::integral_constant<int, m + 4>{});
      if constexpr (ks == 0) acc[i & 1] = MFMA(F[m & 7], Bf[0], f32x16{});
      else acc[i & 1] = MFMA(F[m & 7], Bf[ks], acc[i & 1]);
      if constexpr (i > 0 && (ks == 3 || ks == 5)) { constexpr int s = (ks - 3) >> 1;
        __builtin_amdgcn_raw_buffer_store_b128(pack8(acc[(i - 1) & 1], s), rP, voffP, (i - 1) * 16384 + s * 4096, 16); }
      SBAR();
    });
    __builtin_amdgcn_raw_buffer_store_b128(pack8(acc[0], 0), rP, voffP, 8 * 16384, 16);
    __builtin_amdgcn_raw_buffer_store_b128(pack8(acc[0], 1), rP, voffP, 8 * 16384 + 4096, 16);
    STAMP(CB == 0 ? 5 : 12);
    gld8c(pl, vf, rC, co);
    asm volatile("s_waitcnt vmcnt(0) lgkmcnt(0)\n\ts_barrier" : "+v"(pl) :: "memory");
    if constexpr (CB == 0) STAMP(30);
    signal(rC, co + j * 8, tag, 9, tid);
#pragma unroll
    for (int t = 0; t < 2 * NA; ++t) PIN(pv[t]);
    poll_until(pl, vf, rC, co, tag, 6, j * 8);
    sfor<2 * NA, 12>(ldp);
    gld8c(pl, vf, rC, co);
    sfor<0, NA>(red);
    preload();
    asm volatile("s_waitcnt vmcnt(0)" : "+v"(pl) :: "memory");
#pragma unroll
    for (int t = 2 * NA; t < 12; ++t) PIN(pv[t]);
    poll_until(pl, vf, rC, co, tag, 9, j * 8);
    STAMP(CB == 0 ? 6 : 13);
    sfor<12, 18>(ldp);
    extra();
    sfor<NA, 6>(red);
    sfor<6, 9>([&](auto Tc) AI {
      constexpr int t = decltype(Tc)::value;
      asm volatile("s_waitcnt vmcnt(%2)" : "+v"(pv[2 * t]), "+v"(pv[2 * t + 1]) : "i"(16 - 2 * t + NX) : "memory");
      red(Tc);
    });
  };
  auto rowsum = [&](float v, int slot) AI {
    v += ror8(v); v = swap16_add(v, v); v = swap32_add(v, v);
    if (lane < 8) *(LAS float*)(L + SP_RED + slot * 256 + lane * 32 + w * 4) = v;
    lbar();
    const f32x4 r0 = *(LAS f32x4*)(L + SP_RED + slot * 256 + (lane & 7) * 32), r1 = *(LAS f32x4*)(L + SP_RED + slot * 256 + (lane & 7) * 32 + 16);
    return ((r0[0] + r0[1]) + (r0[2] + r0[3])) + ((r1[0] + r1[1]) + (r1[2] + r1[3]));
  };
  {
    float x2[9], y[9], ga[9], sb[9], cb[9]; u32x4 tbv;
    const unsigned aOwn = lds0 + (17 & 3) * A_SLOT + trb + cp * 8 + hd * 512 + hd * 16384;
    project(std::integral_constant<int, 16>{}, I0{}, Of, aOwn, aOwn ^ 16384,
            [](int i) AI { return (((17 + i) & 3) - ((16 + i) & 3)) * A_SLOT; },
            [](auto Ic) AI { constexpr int i = decltype(Ic)::value;
              constexpr int ops[9] = {4, 6, 6, 6, 6, 6, 4, 6, 2}, NA = XNA;
              constexpr int prev = ops[i - 1] + (NA > 0 && i - 1 == NA + 5 ? 2 * NA : 0);
              return std::integral_constant<int, i == 1 ? 8 : (NA > 0 && i == NA + 5) || i == 8 ? prev : (NA > 0 && i == NA + 2) ? prev + 1 : prev + ops[i > 1 ? i - 2 : 0]>{}; },
            [&](auto Ic, auto Kc) AI {
              constexpr int C = 17 + decltype(Ic)::value, ks = decltype(Kc)::value;
              if constexpr (C < 23) { if constexpr ((ks & 3) == 1) issue(std::integral_constant<int, C + 3>{}, std::integral_constant<int, (ks >> 2)>{}); }
              else if constexpr (C == 23) {
                if constexpr (ks == 1) dma16<0>(m0w1 + SP_X, lane2 * 16 + w * 1024, rXr, (rb * 128 + j * 8) * 1152);
                if constexpr (ks == 5) gld16c<0>(tbv, (w < 5 ? w * 64 + lane2 : 0) * 16, rC, WS_TABB);
                if constexpr (ks == 13) { if (w == 0) dma16<0>(lds0 + SP_X + 8192, lane2 * 16, rXr, (rb * 128 + j * 8) * 1152 + 8192); }
              } else if constexpr (C == 24) { if constexpr (ks % 3 == 1) issueF(I0{}, std::integral_constant<int, ks / 3>{}); }
              else if constexpr (ks == 8) {
                PIN(tbv);
                if (w < 5) *(LAS u32x4*)(L + B_SPARE + (w * 64 + lane2) * 16) = tbv;
              }
            },
            [&]() AI {
              const int e = lane2 >> 3, n0 = 16 * (w >> 1) + 4 * (w & 1) + 8 * (e >> 2) + (e & 3);
              const lptr tb = L + B_SPARE + n0 * 2, xr = L + SP_X + (lane2 & 7) * 1152 + n0 * 2;
#pragma unroll
              for (int t = 0; t < 9; ++t) {
                x2[t] = bflo(*(LAS u16*)(xr + t * 128)); ga[t] = bflo(*(LAS u16*)(tb + t * 128));
                sb[t] = bflo(*(LAS u16*)(tb + 1152 + t * 128)); cb[t] = bflo(*(LAS u16*)(tb + 2 * 1152 + t * 128));
                PIN(x2[t]); PIN(ga[t]); PIN(sb[t]); PIN(cb[t]);
              }
            },
            std::integral_constant<int, 9>{},
            [&]() AI { sfor<0, 5>([&](auto P) AI { issueF(I1{}, P); }); sfor<0, 5>([&](auto P) AI { issueF(std::integral_constant<int, 2>{}, P); }); }, y);
    const int e = lane >> 3, row8 = lane & 7;
    float s = 0.f;
#pragma unroll
    for (int t = 0; t < 9; ++t) { x2[t] += ga[t] * y[t]; s += x2[t]; }
    const float mean = rowsum(s, 0) * (1.f / D_);
    float q = 0.f;
#pragma unroll
    for (int t = 0; t < 9; ++t) { const float d = x2[t] - mean; q += d * d; }
    const float rstd = rsqrtf(rowsum(q, 1) * (1.f / D_) + LN_EPS);
    const lptr sc = L + bslot(3) + w * 1152;
    const lptr xk = L + SP_X + row8 * 1152 + (16 * (w >> 1) + 4 * (w & 1) + 8 * (e >> 2) + (e & 3)) * 2;
#pragma unroll
    for (int t = 0; t < 9; ++t) {
      const float hv = ((x2[t] - mean) * rstd * cb[t] + sb[t]) * LOG2E;
      const unsigned pkd = cvtpk(hv, x2[t]);
      *(LAS u16*)(sc + t * 128 + row8 * 16 + e * 2) = (u16)pkd;
      *(LAS u16*)(xk + t * 128) = (u16)(pkd >> 16);
    }
    asm volatile("s_waitcnt lgkmcnt(0)" ::: "memory");
    const u32x4 c0 = *(LAS u32x4*)(sc + lane * 16), c1 = *(LAS u32x4*)(sc + 1024 + row8 * 16);
    const unsigned ho = (rb * 4 + (j >> 2)) * 36864 + (GU32 ? w * 512 + ((j & 3) * 8 + row8) * 16 : (w >> 2) * 2048 + ((j >> 1) & 1) * 1024 + (w & 3) * 256 + ((j & 1) * 8 + row8) * 16);
    __builtin_amdgcn_raw_buffer_store_b128(c0, rH2, ho + e * 4096, 0, 16);
    if (lane < 8) __builtin_amdgcn_raw_buffer_store_b128(c1, rH2, ho + 8 * 4096, 0, 16);
    STAMP(7);
    if (blockIdx.x == 0 && w < 4) poll_until(u32x2{0u, ~tag[1]}, lane * 8, rC, 7 * 2048 + w * 512, tag, 1, ~0u);
    asm volatile("s_waitcnt vmcnt(0)\n\ts_barrier" ::: "memory");
    signal(rC, 3 * 2048 + rb * 128 + j * 8, tag, 1, tid);
    if (blockIdx.x == 0) signal(rC, WS_EPOCH, tag, 0, tid);
    poll_until(u32x2{0u, ~tag[1]}, (lane & 15) * 8, rC, 3 * 2048 + rb * 128, tag, 1, j * 8);
    STAMP(8);
  }
  bf16x8 F[8], Af[18];
  unsigned lane3 = lane; PIN(lane3);
  const int trb3 = (lane3 >> 5) * 256 + ((lane3 >> 2) & 3) * 64 + (lane3 & 3) * 8, blk3 = (lane3 >> 4) & 1;
  {
    const unsigned voffH = lane3 * 16 + rg * (NKS * 1024);
    u32x4 H[NKS];
    auto issueH = [&](auto Ic) AI { constexpr int i = decltype(Ic)::value;
      gld16c<1024 * (i & 3)>(H[i], voffH, rH2, (i >> 2) * 4096);
    };
    sfor<0, 6>(issueH);
    const unsigned lb = lds0 + bslot(0) + (GU32 ? trb3 + blk3 * 18432 : (blk3 * 4 + ((lane3 >> 2) & 3)) * 64 + (lane3 & 3) * 8 + (lane3 >> 5) * 9216);
    unsigned aE = lb + hf * 512 + ((hf ^ blk3) * 32), aO = lb + hf * 1024 + ((1 ^ hf ^ blk3) * 32);
    PIN(aE); PIN(aO);
    { const unsigned r8v = (lane3 >> 2) & 7;
      voffWd = (288 * j + (GU32 ? 8 * w + r8v : 16 * (w >> 1) + 4 * (w & 1) + 8 * (r8v >> 2) + (r8v & 3))) * 1152 + (lane3 >> 5) * 64 + (lane3 & 3) * 16; }
    f32x4 acc[9][2][2];
    f32x16 acc32[9];
    struct GU { static constexpr int tile(int n) { return n < 216 ? (GU32 ? n % 9 : (n % 18) >> 1) : 3 * ((n / 18 - 12) >> 1) + (n % 18) / 6; }
                static constexpr int kstep(int n) { return n < 216 ? n / 18 : 12 + 3 * ((n / 18) & 1) + ((n % 6) >> 1); }
                static constexpr int k16(int n) { return n < 216 ? n / 9 : 24 + 6 * ((n / 18) & 1) + n % 6; }
                static constexpr int hops(int p) { return p < 9 ? 2 : p < 12 ? 4 : 0; } };
    auto stage = [&](auto Nc) AI {
      constexpr int n = decltype(Nc)::value, C = n / 18, r = n % 18, T = GU::tile(n), mm = n & 1;
      if constexpr (r == 0) {
        if constexpr (C > 0) {
          unsigned d = bslot(C) - bslot(C - 1);
          if constexpr (C == 12 && !GU32) d -= (lane3 >> 5) * 6144;
          aE += d; aO += d; PIN(aE); PIN(aO);
        }
        constexpr int NV = C < 2 ? 4 : 4 + GU::hops(C - 1);
        if constexpr (C < 12) asm volatile("s_waitcnt vmcnt(%2)\n\ts_barrier" : "+v"(H[2 * C]), "+v"(H[2 * C + 1]) : "i"(NV) : "memory");
        else if constexpr (C < 14) { constexpr int b = 24 + 6 * (C - 12);
          asm volatile("s_waitcnt vmcnt(%6)\n\ts_barrier" : "+v"(H[b]), "+v"(H[b + 1]), "+v"(H[b + 2]), "+v"(H[b + 3]), "+v"(H[b + 4]), "+v"(H[b + 5]) : "i"(NV) : "memory"); }
        else asm volatile("s_waitcnt vmcnt(%0)\n\ts_barrier" :: "i"(NV) : "memory");
      }
      constexpr int off = GU32 ? (C < 12 ? 3 * (T / 3) + (T % 3) / 2 + 18 * (r / 9) : (T % 3) / 2 + 6 * (n % 6)) * 512
                               : mm * 18432 + (C < 12 ? 3 * (T / 3) + (T % 3) / 2 : (T % 3) / 2 + 12 * ((n % 6) >> 1)) * 512;
      const lptr b = lp(T % 3 == 1 ? aO : aE) + off;
      F[n & 7] = trfrag(b, b + (C < 12 ? 9 : 3) * 512);
      if constexpr (r % 4 == 0 && r / 4 < GU::hops(C)) issueH(std::integral_constant<int, (C < 9 ? 2 * C + 6 : 4 * C - 12) + r / 4>{});
      if constexpr (C > 0 && (r % 4 == 2 || r == 16)) issueF(std::integral_constant<int, C + 2>{}, std::integral_constant<int, r / 4>{});
      if constexpr (C == 9 && (r % 4 == 3 || r == 17)) { unsigned ln = lane; PIN(ln); setGU(std::integral_constant<int, 3>{}, std::integral_constant<int, r / 4>{}, ln); }
    };
    float e0, e1; unsigned pk[4];
    auto silu = [&](auto Gc, auto Sc) AI {
      constexpr int S = decltype(Sc)::value, u = S / 3, part = S % 3, T = 3 * decltype(Gc)::value + u / 4, rt = (u >> 1) & 1, d = u & 1, q = 2 * (u & 3);
      const float g0 = GU32 ? acc32[T][q] : acc[T][0][rt][2 * d], g1 = GU32 ? acc32[T][q + 1] : acc[T][0][rt][2 * d + 1];
      if constexpr (part == 0) {
        e0 = __builtin_amdgcn_exp2f(-g0); e1 = __builtin_amdgcn_exp2f(-g1);
        PIN(e0); PIN(e1);
      } else if constexpr (part == 1) {
        e0 = __builtin_amdgcn_rcpf(1.f + e0); e1 = __builtin_amdgcn_rcpf(1.f + e1);
        PIN(e0); PIN(e1);
      } else {
        const float u0 = GU32 ? acc32[T][8 + q] : acc[T][1][rt][2 * d], u1 = GU32 ? acc32[T][9 + q] : acc[T][1][rt][2 * d + 1];
        pk[u & 3] = cvtpk(g0 * u0 * e0, g1 * u1 * e1); PIN(pk[u & 3]);
        if constexpr ((u & 3) == 3) { Af[T] = GU32 ? asfrag(u32x4{pk[0], pk[1], pk[2], pk[3]}) : join16(pk[0], pk[1], pk[2], pk[3]); PIN(Af[T]); }
      }
    };
#define GPD 4
    sfor<0, GPD>([&](auto N) AI { stage(N); SBAR(); });
    STAMP(9);
    sfor<0, 324>([&](auto Mc) AI {
      constexpr int m = decltype(Mc)::value, T = GU::tile(m), ks = GU::kstep(m), mm = m & 1;
      if constexpr (GU32) { if constexpr (m < 9) acc32[T] = MFMA(F[m & 7], asfrag(H[GU::k16(m)]), f32x16{}); else acc32[T] = MFMA(F[m & 7], asfrag(H[GU::k16(m)]), acc32[T]); }
      else mfma16<(m < 18)>(acc[T][mm][0], F[m & 7], H[2 * ks]);
      if constexpr (m + GPD < 324) stage(std::integral_constant<int, m + GPD>{});
      if constexpr (m >= 241 && m < 313) silu(std::integral_constant<int, (m - 241) / 36>{}, std::integral_constant<int, (m - 241) % 36>{});
      if constexpr (m >= 313) silu(std::integral_constant<int, 2>{}, std::integral_constant<int, 2 * (m - 313)>{});
      if constexpr (!GU32) { SBAR(); mfma16<(m < 18)>(acc[T][mm][1], F[m & 7], H[2 * ks + 1]); }
      if constexpr (m >= 313) silu(std::integral_constant<int, 2>{}, std::integral_constant<int, 2 * (m - 313) + 1>{});
      SBAR();
    });
    STAMP(10);
    asm volatile("s_nop 7"); SBAR();
    lbar();
    const lptr xw = L + bslot(16) + w * 9216 + lane3 * 16, xr = L + bslot(16) + (w ^ 4) * 9216 + lane3 * 16;
#pragma unroll
    for (int t = 0; t < 7; ++t) *(LAS bf16x8*)(xw + t * 1024) = Af[t];
    sfor<22, 36>([&](auto Sc) AI { silu(std::integral_constant<int, 2>{}, Sc); });
    *(LAS bf16x8*)(xw + 7 * 1024) = Af[7]; *(LAS bf16x8*)(xw + 8 * 1024) = Af[8];
    wait_bar<0>();
#pragma unroll
    for (int t = 0; t < 9; ++t) Af[9 + t] = ldfrag(xr + t * 1024);
    lbar();
    SBAR();
    sfor<0, 5>([&](auto P) AI { issueF(std::integral_constant<int, 20>{}, P); });
  }
  STAMP(11);
  {
    float y[9], gb[9], x2[9];
    const unsigned aT = lds0 + bslot(18) + trb3 + blk3 * 32 + hf * 512;
    project(std::integral_constant<int, 18>{}, std::integral_constant<int, 4>{}, Af, aT + hf * 6144, aT + (1 - hf) * 6144,
            [](int i) AI { return bslot(18 + i) - bslot(17 + i); },
            [](auto Ic) AI { constexpr int i = decltype(Ic)::value;
              constexpr int ops[9] = {4, 6, 6, 6, 6, 6, 2, 2, 2}, NA = XNA;
              constexpr int prev = ops[i - 1] + (NA > 0 && i - 1 == NA + 5 ? 2 * NA : 0);
              return std::integral_constant<int, i == 1 ? 8 : (NA > 0 && i == NA + 5) || i == 8 ? prev : (NA > 0 && i == NA + 2) ? prev + 1 : prev + ops[i > 1 ? i - 2 : 0]>{}; },
            [&](auto Ic, auto Kc) AI {
              constexpr int C = 18 + decltype(Ic)::value, ks = decltype(Kc)::value;
              if constexpr (ks >= 1 && ks < 16 && (ks % 3) == 1) issueF(std::integral_constant<int, C + 3>{}, std::integral_constant<int, (ks / 3)>{});
            },
            [&]() AI {
              const int e = lane3 >> 3, n0 = 16 * (w >> 1) + 4 * (w & 1) + 8 * (e >> 2) + (e & 3);
#pragma unroll
              for (int t = 0; t < 9; ++t) {
                gb[t] = bflo(*(LAS u16*)(L + B_SPARE + 3 * 1152 + n0 * 2 + t * 128)); x2[t] = bflo(*(LAS u16*)(L + SP_X + (lane3 & 7) * 1152 + n0 * 2 + t * 128));
                PIN(gb[t]); PIN(x2[t]);
              }
            },
            I0{}, [&]() AI {}, y);
    unsigned ln = lane; PIN(ln);
    const int e = ln >> 3, row8 = ln & 7, n0 = 16 * (w >> 1) + 4 * (w & 1) + 8 * (e >> 2) + (e & 3);
    const lptr im = L + row8 * 1152 + n0 * 2;
#pragma unroll
    for (int t = 0; t < 9; ++t) {
      const float ov = x2[t] + gb[t] * (1.f / (LOG2E * LOG2E)) * y[t];
      *(LAS u16*)(im + t * 128) = (u16)cvtpk(ov, ov);
    }
    lbar();
    u16* op = out + (size_t)(rb * 128 + j * 8) * D_;
    const unsigned t2 = w * 64 + ln;
    *(u32x4*)(op + t2 * 8) = *(LAS u32x4*)(L + t2 * 16);
    if (w == 0) *(u32x4*)(op + 4096 + ln * 8) = *(LAS u32x4*)(L + 8192 + ln * 16);
  }
  asm volatile(".Lkm_end:");
  STAMP(14);
}

extern "C" void kernel_launch(void* const* d_in, const int* in_sizes, int n_in, void* d_out, int out_size, void* d_ws, size_t ws_size,
                              hipStream_t stream) {
  (void)in_sizes; (void)n_in; (void)out_size; (void)ws_size;
  const u16* x = (const u16*)d_in[0]; const u16* kc = (const u16*)d_in[1]; const u16* vc = (const u16*)d_in[2];
  const u16* wq = (const u16*)d_in[3]; const u16* wo = (const u16*)d_in[4];
  const u16* wg = (const u16*)d_in[6]; const u16* wu = (const u16*)d_in[7]; const u16* wd = (const u16*)d_in[8];
  const u16* c = (const u16*)d_in[9]; const u16* wmod = (const u16*)d_in[10];
  char* ws = (char*)d_ws;
  (void)hipFuncSetAttribute((const void*)kmain, hipFuncAttributeMaxDynamicSharedMemorySize, A_LDS);
  kmain<<<256, 512, A_LDS, stream>>>(x, wq, kc, vc, wo, wg, wu, wd, c, wmod, ws, (u16*)d_out);
}
```

```cpp
#include <hip/hip_runtime.h>
#include <stdint.h>
#include <type_traits>

typedef short    bf16x8 __attribute__((ext_vector_type(8)));
typedef short    s16x4  __attribute__((ext_vector_type(4)));
typedef float    f32x16 __attribute__((ext_vector_type(16)));
typedef float    f32x4  __attribute__((ext_vector_type(4)));
typedef float    f32x2  __attribute__((ext_vector_type(2)));
typedef __bf16   bf16x2 __attribute__((ext_vector_type(2)));
typedef unsigned u32x4  __attribute__((ext_vector_type(4)));
typedef unsigned u32x2  __attribute__((ext_vector_type(2)));
typedef unsigned short u16;
#define LAS __attribute__((address_space(3)))
typedef LAS char* lptr;
typedef __amdgpu_buffer_rsrc_t rsrc_t;
#define DI __device__ __forceinline__
#define AI __attribute__((always_inline))
#define SBAR() __builtin_amdgcn_sched_barrier(0)
#define PIN(x) asm volatile("" : "+v"(x))
#define MFMA(a, b, c) __builtin_amdgcn_mfma_f32_32x32x16_bf16((a), (b), (c), 0, 0, 0)

constexpr int S_ = 2048, D_ = 576, HQ = 32, HKV = 16, SK = 2048, DH = 128, WIN = 512, F_ = 4608;
constexpr int NKS = D_ / 16;
constexpr int NRG = S_ / 32;
constexpr float LN_EPS = 1e-5f, LOG2E = 1.4426950408889634f;
constexpr float QSCALE = 0.08838834764831845f * 1.4426950408889634f;

constexpr unsigned WS_FLAG = 0;
constexpr unsigned WS_EPOCH = 16384;
constexpr unsigned WS_GRAN = 20480;
constexpr unsigned WS_TABB = 49152;
constexpr size_t WS_XH   = 65536;
constexpr size_t ACT_B   = (size_t)S_ * D_ * 2;
constexpr size_t WS_H2   = WS_XH + ACT_B;
constexpr size_t WS_X2   = WS_H2 + ACT_B;
constexpr size_t WS_P    = WS_X2 + 2 * ACT_B;
constexpr size_t WS_P2   = WS_P;
constexpr size_t WS_DBG  = WS_P + 32 * ACT_B;

DI unsigned rfl(unsigned x) { return __builtin_amdgcn_readfirstlane(x); }
DI unsigned cvtpk(float lo, float hi) { f32x2 v = {lo, hi}; bf16x2 b = __builtin_convertvector(v, bf16x2); return __builtin_bit_cast(unsigned, b); }
DI float bflo(unsigned w) { return __uint_as_float(w << 16); }
DI float bfhi(unsigned w) { return __uint_as_float(w & 0xffff0000u); }
DI float bfr(float x) { return bflo(cvtpk(x, x)); }
DI rsrc_t mkrsrc(const void* p, unsigned bytes) { return __builtin_amdgcn_make_buffer_rsrc((void*)p, (short)0, (int)bytes, 0x00020000); }
template <int OFF> DI void dma16(unsigned m0v, unsigned voff, rsrc_t r, unsigned soff) {
  asm volatile("s_mov_b32 m0, %0\n\ts_nop 0\n\tbuffer_load_dwordx4 %1, %2, %3 offen offset:%4 lds"
               :: "s"(m0v), "v"(voff), "s"(r), "s"(soff), "i"(OFF) : "memory");
}
DI void touch(unsigned m0v, unsigned voff, rsrc_t r) {
  asm volatile("s_mov_b32 m0, %0\n\ts_nop 0\n\tbuffer_load_dword %1, %2, 0 offen lds" :: "s"(m0v), "v"(voff), "s"(r) : "memory");
}
template <int OFF> DI void gld16(u32x4& d, unsigned voff, rsrc_t r, unsigned soff) {
  asm volatile("buffer_load_dwordx4 %0, %1, %2, %3 offen offset:%4" : "=v"(d) : "v"(voff), "s"(r), "s"(soff), "i"(OFF) : "memory");
}
template <int OFF> DI void gld16c(u32x4& d, unsigned voff, rsrc_t r, unsigned soff) {
  asm volatile("buffer_load_dwordx4 %0, %1, %2, %3 offen offset:%4 sc1" : "=v"(d) : "v"(voff), "s"(r), "s"(soff), "i"(OFF) : "memory");
}
template <bool FIRST> DI void mfma16(f32x4& c, bf16x8 a, u32x4 b) {
  if constexpr (FIRST) asm volatile("v_mfma_f32_16x16x32_bf16 %0, %1, %2, 0" : "=v"(c) : "v"(a), "v"(b));
  else asm volatile("v_mfma_f32_16x16x32_bf16 %0, %1, %2, %0" : "+v"(c) : "v"(a), "v"(b));
}
DI void prio_up(unsigned cond) { asm volatile("s_cmp_eq_u32 %0, 0\n\ts_cbranch_scc1 1f\n\ts_setprio 1\n1:" :: "s"(cond) : "scc"); }
DI s16x4 trd(lptr p) { return __builtin_amdgcn_ds_read_tr16_b64_v4i16((LAS s16x4*)p); }
DI bf16x8 trfrag(lptr p0, lptr p1) { s16x4 lo = trd(p0), hi = trd(p1); return __builtin_shufflevector(lo, hi, 0, 1, 2, 3, 4, 5, 6, 7); }
DI bf16x8 ldfrag(lptr p) { return *(LAS bf16x8*)p; }
DI bf16x8 asfrag(u32x4 w) { return __builtin_bit_cast(bf16x8, w); }
DI float swap_max(float m) { auto rr = __builtin_amdgcn_permlane32_swap(__float_as_uint(m), __float_as_uint(m), false, false);
  return __builtin_fmaxf(__uint_as_float(rr[0]), __uint_as_float(rr[1])); }
DI float swap_add(float m) { auto rr = __builtin_amdgcn_permlane32_swap(__float_as_uint(m), __float_as_uint(m), false, false);
  return __uint_as_float(rr[0]) + __uint_as_float(rr[1]); }
DI float swap16_add(float a, float b) { auto rr = __builtin_amdgcn_permlane16_swap(__float_as_uint(a), __float_as_uint(b), false, false);
  return __uint_as_float(rr[0]) + __uint_as_float(rr[1]); }
DI float swap32_add(float a, float b) { auto rr = __builtin_amdgcn_permlane32_swap(__float_as_uint(a), __float_as_uint(b), false, false);
  return __uint_as_float(rr[0]) + __uint_as_float(rr[1]); }
DI float ror8(float a) { return __int_as_float(__builtin_amdgcn_update_dpp(0, __float_as_int(a), 0x128, 0xf, 0xf, false)); }
DI void lbar() { asm volatile("s_waitcnt lgkmcnt(0)\n\ts_barrier" ::: "memory"); }
DI void signal(rsrc_t r, unsigned soff, u32x2 tag, unsigned prog, int tid) { if (tid == 0) __builtin_amdgcn_raw_buffer_store_b64(u32x2{tag[0] | prog, tag[1]}, r, 0, soff, 16); }
DI void gld8c(u32x2& v, unsigned voff, rsrc_t r, unsigned soff) { asm volatile("buffer_load_dwordx2 %0, %1, %2, %3 offen sc1" : "=v"(v) : "v"(voff), "s"(r), "s"(soff) : "memory"); }
DI bool all_lanes(bool c) { return __builtin_amdgcn_ballot_w64(c) == ~0ull; }
DI bool flag_ok(u32x2 v, u32x2 tag, unsigned need) { return v[1] == tag[1] && v[0] - (tag[0] + need) <= 15u - need; }
DI void poll_until(u32x2 v, unsigned voff, rsrc_t r, unsigned soff, u32x2 tag, unsigned need, unsigned own) {
  for (int sp = 0; !all_lanes(flag_ok(v, tag, need) || voff == own) && sp < (1 << 20); ++sp) {
    __builtin_amdgcn_s_sleep(2);
    asm volatile("buffer_load_dwordx2 %0, %1, %2, %3 offen sc1\n\ts_waitcnt vmcnt(0)" : "=v"(v) : "v"(voff), "s"(r), "s"(soff) : "memory");
  }
}
DI void wait_group(unsigned w, unsigned voff, rsrc_t r, unsigned soff, u32x2 tag, unsigned need, unsigned own) {
  if (w == 0) {
    const unsigned long long t = ((unsigned long long)tag[1] << 32) | tag[0], lo = t + need, hi = t + 15, om = __builtin_amdgcn_ballot_w64(voff == own);
    u32x2 a, b, c; unsigned n; unsigned long long m;
#define WG_LD(x) "buffer_load_dwordx2 " x ", %5, %6, %7 offen sc1\n\t"
#define WG_CK(x) "s_waitcnt vmcnt(2)\n\tv_cmp_le_u64 vcc, %8, " x "\n\tv_cmp_ge_u64 %4, %9, " x "\n\ts_and_b64 vcc, vcc, %4\n\ts_or_b64 vcc, vcc, %10\n\ts_cmp_eq_u64 vcc, -1\n\ts_cbranch_scc1 2f\n\t" WG_LD(x)
    asm volatile(WG_LD("%0") "s_sleep 6\n\t" WG_LD("%1") "s_sleep 6\n\t" WG_LD("%2") "s_mov_b32 %3, 0x80000\n1:\n\t"
                 WG_CK("%0") WG_CK("%1") WG_CK("%2") "s_sub_u32 %3, %3, 1\n\ts_cbranch_scc0 1b\n2:\n\ts_barrier\n\ts_waitcnt vmcnt(0)"
                 : "=&v"(a), "=&v"(b), "=&v"(c), "=&s"(n), "=&s"(m) : "v"(voff), "s"(r), "s"(soff), "s"(lo), "s"(hi), "s"(om) : "vcc", "scc", "memory");
#undef WG_LD
#undef WG_CK
  } else asm volatile("s_barrier" ::: "memory");
}
DI bf16x8 join16(unsigned a0, unsigned a1, unsigned b0, unsigned b1) {
  auto r0 = __builtin_amdgcn_permlane16_swap(a0, b0, false, false); auto r1 = __builtin_amdgcn_permlane16_swap(a1, b1, false, false);
  return __builtin_bit_cast(bf16x8, u32x4{r0[0], r1[0], r0[1], r1[1]});
}
DI u32x4 pack8(const f32x16& a, int s) {
  u32x4 w = {cvtpk(a[8 * s], a[8 * s + 1]), cvtpk(a[8 * s + 2], a[8 * s + 3]), cvtpk(a[8 * s + 4], a[8 * s + 5]), cvtpk(a[8 * s + 6], a[8 * s + 7])};
  return w;
}
DI lptr lp(unsigned a) { return (lptr)(uintptr_t)a; }
DI void bump(unsigned& a, int d) { a += d; PIN(a); }
template <int N> DI void wait_bar() { asm volatile("s_waitcnt vmcnt(%0) lgkmcnt(0)\n\ts_barrier" :: "i"(N) : "memory"); }
template <int I, int N, class F> DI void sfor(F&& f) { if constexpr (I < N) { f(std::integral_constant<int, I>{}); sfor<I + 1, N>(f); } }

#define STAMP(i)

constexpr int A_SLOT = 32768, A_SPARE = 4 * A_SLOT, A_LDS = 5 * A_SLOT, A_JUNK = A_SPARE + 16384;
constexpr float L_LIMIT = 1.2379400392853803e27f;
constexpr float M_MARGIN = 40.f;

#define XNA 3
#define GU32 0
DI unsigned ncol(unsigned w, unsigned ln) { return 64 * (ln >> 5) + 16 * (w >> 1) + 4 * (w & 1) + 8 * ((ln >> 4) & 1) + 2 * ((ln >> 3) & 1); }
struct AttnState {
  f32x16 O[4], S[2], negm; u32x4 Pk[2][2]; bf16x8 Q[8], F[8]; float l;
};

constexpr int B_SLOT = 36864, B_SPARE = 4 * B_SLOT, SP_X = B_SPARE + 5120, SP_RED = B_SPARE + 14336;
constexpr int aslot(int C) { return (C < 19 ? C & 3 : (C & 3) == 3 ? 0 : (C & 3) == 0 ? 3 : C & 3) * A_SLOT; }
constexpr int bslot(int C) { return ((C + 2) & 3) * B_SLOT; }

__global__ __launch_bounds__(512, 2) void kmain(const u16* __restrict__ x, const u16* __restrict__ wq, const u16* __restrict__ kc,
                                                const u16* __restrict__ vc, const u16* __restrict__ wo, const u16* __restrict__ wg,
                                                const u16* __restrict__ wu, const u16* __restrict__ wd, const u16* __restrict__ cvec,
                                                const u16* __restrict__ wmod, char* __restrict__ ws, u16* __restrict__ out) {
  const u16* xh = (const u16*)(ws + WS_XH);
  extern __shared__ __attribute__((aligned(16))) char smem[];
  const lptr L = (lptr)smem;
  const unsigned lds0 = (unsigned)(uintptr_t)L;
  const int tid = threadIdx.x, lane = tid & 63;
  const unsigned w = rfl(tid >> 6), g = w & 3, hd = w >> 2;
  const unsigned j = 2 * (blockIdx.x & 7) + ((blockIdx.x >> 3) & 1), rb = blockIdx.x >> 4, rg = rb * 4 + g;
  const int r32 = lane & 31, h = lane >> 5, r8 = (lane >> 2) & 7, cp = lane & 3, blk = (lane >> 4) & 1, q4 = (lane >> 2) & 3;
  STAMP(0);
  const rsrc_t rWq = mkrsrc(wq, D_ * 4096 * 2), rK = mkrsrc(kc, HKV * SK * DH * 2), rV = mkrsrc(vc, HKV * SK * DH * 2),
               rWo = mkrsrc(wo, 4096 * D_ * 2), rX = mkrsrc(xh, S_ * D_ * 2);
  const unsigned voffWq = (16 * (w >> 1) + 8 * (r8 >> 2) + 4 * (w & 1) + (r8 & 3)) * 8192 + h * 64 + ((cp ^ ((r8 >> 2) * 2)) * 16) + 512 * j;
  const unsigned voffX = lane * 16 + rg * (NKS * 1024);
  const unsigned kvrow = 8 * (w >> 1) + r8;
  const unsigned voffV = kvrow * 256 + (2 * (w & 1) + h) * 64 + cp * 16;
  const unsigned voffK = kvrow * 256 + (2 * (w & 1) + h) * 64 + ((cp ^ ((kvrow >> 2) & 3)) * 16);
  const unsigned kvbase = (j * SK + (SK - WIN)) * 256;
  const unsigned voffWo = (256 * j + 32 * w + r8) * 1152 + h * 64 + cp * 16;
  const unsigned m0w4 = lds0 + w * 4096, m0w1 = lds0 + w * 1024;

  auto issue = [&](auto Cc, auto Pc) AI {
    constexpr int C = decltype(Cc)::value, P = decltype(Pc)::value, slot = aslot(C);
    if constexpr (C < 9) dma16<128 * P>(m0w4 + slot + P * 896, voffWq, rWq, C * 64 * 8192);
    else if constexpr (C < 17) {
      constexpr int i = C - 9;
      constexpr bool isK = (P < 2 && !(i == 7 && P == 1)) || (i == 0 && P == 2);
      constexpr int u = (i == 0 && P == 2) ? 0 : (i == 7 && P == 1) ? 15 : (P < 2 ? 2 * i + 1 + P : 2 * i - 3 + P);
      if constexpr (isK) dma16<0>(m0w1 + slot + P * 8192, voffK, rK, kvbase + u * 8192);
      else               dma16<0>(m0w1 + slot + P * 8192, voffV, rV, kvbase + u * 8192);
    } else if constexpr (C < 26) dma16<0>(m0w4 + slot + P * 1024, voffWo, rWo, (C - 17) * 128 + P * 8 * 1152);
  };
  u32x4 X[NKS];
  auto issueX = [&](auto Cc, auto Pc) AI {
    constexpr int C = decltype(Cc)::value, P = decltype(Pc)::value;
    if constexpr (C < 9) gld16c<1024 * P>(X[4 * C + P], voffX, rX, C * 4096);
  };
  const rsrc_t rC = mkrsrc(ws, 65536), rXr = mkrsrc(x, S_ * D_ * 2);
  const unsigned voffF = (lane & 15) * 8;
  unsigned long long pc0; unsigned clen;
  asm volatile("s_getpc_b64 %0\n.Lkm_begin:\n\ts_mov_b32 %1, .Lkm_end-.Lkm_begin" : "=s"(pc0), "=s"(clen));
  const rsrc_t rCode = mkrsrc((const void*)pc0, clen);
  if (w == 7) touch(lds0 + A_JUNK, ((blockIdx.x >> 3) * 32 + (lane & 31)) * 128, rCode);
  u32x2 tag;
  {
    const int e = lane >> 3, row8 = lane & 7;
    u32x2 ev; u32x4 wm[3], xa, xb; unsigned cv[3];
    asm volatile("buffer_load_dwordx2 %0, off, %1, %2 sc1" : "=v"(ev) : "s"(rC), "s"(WS_EPOCH) : "memory");
    dma16<0>(m0w1 + A_SPARE + 4096, e * 1152 + row8 * 16 + w * 128, rXr, (rb * 128 + j * 8) * 1152);
    gld16<1024>(xb, row8 * 1152 + w * 16, rXr, (rb * 128 + j * 8) * 1152);
    const rsrc_t rM = mkrsrc(wmod, D_ * 3456 * 2), rCv = mkrsrc(cvec, D_ * 2);
    const unsigned pb = ((blockIdx.x & 7) + 8 * (blockIdx.x >> 5)) * 4 + ((blockIdx.x >> 3) & 3);
    const unsigned vm = (tid >> 1) * 6912 + (tid & 1) * 16 + pb * 32;
#pragma unroll
    for (int i = 0; i < 3; ++i) {
      gld16<0>(wm[i], vm, rM, i * 256 * 6912);
      asm volatile("buffer_load_ushort %0, %1, %2, %3 offen" : "=v"(cv[i]) : "v"(tid & ~1), "s"(rCv), "s"(i * 512) : "memory");
    }
    asm volatile("s_waitcnt vmcnt(0)" ::: "memory");
    if (w >= 6) {
      const unsigned u = tid - 384;
      touch(lds0 + A_JUNK, (36 * rb + (u >> 2)) * 8192 + 512 * j + (u & 3) * 128, rWq);
      if (w == 6) touch(lds0 + A_JUNK, (36 * rb + 32 + ((lane & 15) >> 2)) * 8192 + 512 * j + (u & 3) * 128, rWq);
    }
    asm volatile("" : "+v"(ev), "+v"(wm[0]), "+v"(wm[1]), "+v"(wm[2]), "+v"(cv[0]), "+v"(cv[1]), "+v"(cv[2]), "+v"(xb) :: "memory");
    xa = *(LAS u32x4*)(L + A_SPARE + 4096 + w * 1024 + row8 * 128 + e * 16);
    STAMP(16);
    { const unsigned long long t = (((unsigned long long)rfl(ev[1]) << 32) | rfl(ev[0])) + 16ull; tag[0] = (unsigned)t & ~15u; tag[1] = (unsigned)(t >> 32); }
    const unsigned gl = tag[0] << 12, gh = (tag[1] << 12) | (tag[0] >> 20);
    float a[8];
#pragma unroll
    for (int q = 0; q < 8; ++q) a[q] = 0.f;
#pragma unroll
    for (int i = 0; i < 3; ++i) {
      const float c0 = bflo(cv[i]), sc = bfr(c0 * __builtin_amdgcn_rcpf(1.f + __builtin_amdgcn_exp2f(c0 * -1.4426950408889634f)));
#pragma unroll
      for (int q = 0; q < 4; ++q) { a[2 * q] += sc * bflo(wm[i][q]); a[2 * q + 1] += sc * bfhi(wm[i][q]); }
    }
    const bool b3 = e & 1;
    const float a0 = swap32_add(a[0], a[4]), a1 = swap32_add(a[1], a[5]), a2 = swap32_add(a[2], a[6]), a3 = swap32_add(a[3], a[7]);
    const float b0 = swap16_add(a0, a2), b1 = swap16_add(a1, a3);
    float ms = (b3 ? b1 : b0) + ror8(b3 ? b0 : b1);
    ms += __shfl_xor(ms, 2); ms += __shfl_xor(ms, 4);
    const lptr red = L + A_SPARE;
    if ((lane & 6) == 0) *(LAS float*)(red + ((lane & 1) * 8 + e) * 32 + w * 4) = ms;
    float v[16];
#pragma unroll
    for (int q = 0; q < 4; ++q) { v[2 * q] = bflo(xa[q]); v[2 * q + 1] = bfhi(xa[q]); v[8 + 2 * q] = bflo(xb[q]); v[9 + 2 * q] = bfhi(xb[q]); }
    float s0 = 0.f, s1 = 0.f;
#pragma unroll
    for (int q = 0; q < 8; ++q) { s0 += v[q]; s1 += v[8 + q]; }
    s0 += e == 0 ? s1 : 0.f;
    s0 += ror8(s0); s0 = swap16_add(s0, s0); s0 = swap32_add(s0, s0);
    if (lane < 8) *(LAS float*)(red + 512 + lane * 32 + w * 4) = s0;
    lbar();
    auto sum8 = [&](lptr q) AI { const f32x4 r0 = *(LAS f32x4*)q, r1 = *(LAS f32x4*)(q + 16);
      return ((r0[0] + r0[1]) + (r0[2] + r0[3])) + ((r1[0] + r1[1]) + (r1[2] + r1[3])); };
    if (tid < 16 && pb < 216) {
      float m = bfr(sum8(red + tid * 32));
      const unsigned vec = pb / 36;
      if (vec == 1 || vec == 4) m = bfr(1.f + m);
      if (vec >= 2) {
        __builtin_amdgcn_raw_buffer_store_b16((u16)(__float_as_uint(m) >> 16), rC, tid * 2, WS_TABB + (pb - 72) * 32, 16);
        asm volatile("s_waitcnt vmcnt(0)" ::: "memory");
      }
      const u32x2 gr = {gl | (__float_as_uint(m) >> 16), gh};
      __builtin_amdgcn_raw_buffer_store_b64(gr, rC, tid * 8, WS_GRAN + pb * 128, 16);
    }
    STAMP(17);
    const float mean = sum8(red + 512 + row8 * 32) * (1.f / D_);
    s0 = 0.f; s1 = 0.f;
#pragma unroll
    for (int q = 0; q < 8; ++q) { v[q] -= mean; v[8 + q] -= mean; s0 += v[q] * v[q]; s1 += v[8 + q] * v[8 + q]; }
    s0 += e == 0 ? s1 : 0.f;
    s0 += ror8(s0); s0 = swap16_add(s0, s0); s0 = swap32_add(s0, s0);
    if (lane < 8) *(LAS float*)(red + 768 + lane * 32 + w * 4) = s0;
#define MIDC 2
    sfor<0, MIDC>([&](auto C) AI { sfor<0, 4>([&](auto P) AI { issue(C, P); }); });
    u32x2 g0, g1, g2;
    const lptr tab = red + 1024;
    if (w < 6) {
      for (int sp = 0; sp < (1 << 20); ++sp) {
        asm volatile("buffer_load_dwordx2 %0, %3, %4, %5 offen sc1\n\tbuffer_load_dwordx2 %1, %3, %4, %5 offen offset:3072 sc1\n\tbuffer_load_dwordx2 %2, %3, %4, %6 offen sc1\n\ts_waitcnt vmcnt(0)"
                     : "=&v"(g0), "=&v"(g1), "=&v"(g2) : "v"(tid * 8), "s"(rC), "s"(WS_GRAN), "s"(WS_GRAN + 6144) : "memory");
        if (all_lanes((((g0[0] ^ gl) | (g1[0] ^ gl) | (g2[0] ^ gl)) >> 16 | (g0[1] ^ gh) | (g1[1] ^ gh) | (g2[1] ^ gh)) == 0u)) break;
        __builtin_amdgcn_s_sleep(1);
      }
      *(LAS u16*)(tab + tid * 2) = (u16)g0[0]; *(LAS u16*)(tab + 768 + tid * 2) = (u16)g1[0]; *(LAS u16*)(tab + 1536 + tid * 2) = (u16)g2[0];
    }
    lbar();
    STAMP(15);
    const float rs = rsqrtf(sum8(red + 768 + row8 * 32) * (1.f / D_) + LN_EPS);
    const u32x4 sa = *(LAS u32x4*)(tab + w * 128 + e * 16), ca = *(LAS u32x4*)(tab + 1152 + w * 128 + e * 16);
    const u32x4 sb = *(LAS u32x4*)(tab + 1024 + w * 16), cb = *(LAS u32x4*)(tab + 1152 + 1024 + w * 16);
    u32x4 ha, hb;
#pragma unroll
    for (int q = 0; q < 4; ++q) {
      ha[q] = cvtpk((v[2 * q] * rs * bflo(ca[q]) + bflo(sa[q])) * QSCALE, (v[2 * q + 1] * rs * bfhi(ca[q]) + bfhi(sa[q])) * QSCALE);
      hb[q] = cvtpk((v[8 + 2 * q] * rs * bflo(cb[q]) + bflo(sb[q])) * QSCALE, (v[9 + 2 * q] * rs * bfhi(cb[q]) + bfhi(sb[q])) * QSCALE);
    }
    const unsigned ho = (rb * 4 + (j >> 2)) * 36864 + ((j >> 1) & 1) * 1024 + ((j & 1) * 8 + row8) * 16;
    __builtin_amdgcn_raw_buffer_store_b128(ha, rX, ho + (2 * w + (e >> 2)) * 2048 + (e & 3) * 256, 0, 16);
    if (lane < 8) __builtin_amdgcn_raw_buffer_store_b128(hb, rX, ho + (16 + (w >> 2)) * 2048 + (w & 3) * 256, 0, 16);
    asm volatile("s_waitcnt vmcnt(0)\n\ts_barrier" ::: "memory");
    STAMP(18);
    signal(rC, 7 * 2048 + rb * 128 + j * 8, tag, 1, tid);
    sfor<MIDC, 4>([&](auto C) AI { sfor<0, 4>([&](auto P) AI { issue(C, P); }); });
    if (w >= 1 && w < 4) {
      const unsigned vg = (tid < 208 ? tid - 64 : 0) * 128;
      for (int sp = 0; sp < (1 << 20); ++sp) {
        asm volatile("buffer_load_dwordx2 %0, %1, %2, %3 offen sc1\n\ts_waitcnt vmcnt(0)" : "=&v"(g1) : "v"(vg), "s"(rC), "s"(WS_GRAN + 1152 * 8) : "memory");
        if (all_lanes(((g1[0] ^ gl) >> 16 | (g1[1] ^ gh)) == 0u)) break;
        __builtin_amdgcn_s_sleep(1);
      }
    }
    wait_group(w, voffF, rC, 7 * 2048 + rb * 128, tag, 1, j * 8);
    STAMP(19);
  }
  sfor<0, 3>([&](auto C) AI { sfor<0, 4>([&](auto P) AI { issueX(C, P); }); });

  const int trb = h * 256 + q4 * 64 + blk * 32;
  unsigned aWq = lds0 + h * 8192 + (blk * 4 + q4) * 64 + blk * 32 + cp * 8 + hd * 2048, aWq1 = aWq ^ 32;
  PIN(aWq); PIN(aWq1);
  AttnState st;
  {
    f32x4 acc[8][2];
    auto stage = [&](auto Nc) AI {
      constexpr int n = decltype(Nc)::value, T = n & 7, C = n >> 4, ksl = (n >> 3) & 1;
      if constexpr ((n & 15) == 0) {
        if constexpr (C > 0) { bump(aWq, ((C & 3) - ((C - 1) & 3)) * A_SLOT); bump(aWq1, ((C & 3) - ((C - 1) & 3)) * A_SLOT); }
        constexpr int NV = C == 3 ? 12 : C == 8 ? 4 : 8;
        if constexpr (C >= 6) asm volatile("s_waitcnt lgkmcnt(0)" ::: "memory");
        asm volatile("s_waitcnt vmcnt(%4)\n\ts_barrier" : "+v"(X[4 * C]), "+v"(X[4 * C + 1]), "+v"(X[4 * C + 2]), "+v"(X[4 * C + 3]) : "i"(NV) : "memory");
      }
      const lptr b = lp((T & 1) ? aWq1 : aWq) + (4 * ksl * 8 + (T >> 1)) * 512;
      st.F[n & 7] = trfrag(b, b + 4096);
      if constexpr (n & 1) {
        using PC = std::integral_constant<int, ((n & 15) >> 2)>;
        if constexpr (n & 2) { if constexpr (C > 1 && C < 7) issue(std::integral_constant<int, C + 2>{}, PC{}); }
        else if constexpr (C < 6) issueX(std::integral_constant<int, C + 3>{}, PC{});
        else issue(std::integral_constant<int, C + 3>{}, PC{});
      }
    };
    sfor<0, 4>([&](auto N) AI { stage(N); SBAR(); });
    STAMP(1);
    sfor<0, 144>([&](auto Mc) AI {
      constexpr int m = decltype(Mc)::value;
      mfma16<(m < 8)>(acc[m & 7][0], st.F[m & 7], X[2 * (m >> 3)]);
      if constexpr (m + 4 < 144) stage(std::integral_constant<int, m + 4>{});
      mfma16<(m < 8)>(acc[m & 7][1], st.F[m & 7], X[2 * (m >> 3) + 1]);
      SBAR();
    });
    asm volatile("s_nop 7"); SBAR();
#pragma unroll
    for (int t = 0; t < 8; ++t)
      st.Q[t] = join16(cvtpk(acc[t][0][0], acc[t][0][1]), cvtpk(acc[t][0][2], acc[t][0][3]), cvtpk(acc[t][1][0], acc[t][1][1]), cvtpk(acc[t][1][2], acc[t][1][3]));
  }
  STAMP(2);
  unsigned aK0 = lds0 + (9 & 3) * A_SLOT + 2048 * (r32 >> 3) + 64 * (r32 & 7) + 16 * ((0 + h) ^ ((r32 >> 2) & 3));
  unsigned aK1 = aK0 ^ 32, aV = lds0 + (9 & 3) * A_SLOT + trb + cp * 8;
  PIN(aK0); PIN(aK1); PIN(aV);
#pragma unroll
  for (int t = 0; t < 4; ++t) st.O[t] = f32x16{};
  auto kfrag = [&](int off, auto Ic) AI { constexpr int i = decltype(Ic)::value; return ldfrag(lp((i & 1) ? aK1 : aK0) + off + 512 * (i >> 1)); };
  auto vfrag = [&](int off, auto Ic) AI { constexpr int i = decltype(Ic)::value, ks = i >> 2, T = i & 3;
    return trfrag(lp(aV) + off + ((2 * ks) * 4 + T) * 512, lp(aV) + off + ((2 * ks + 1) * 4 + T) * 512); };
  auto step = [&](auto Par, auto HQK, auto HPV, auto NXT, auto NVc, int u, int ko, int vo, int kn, int dsl, auto&& dmaf) AI {
    constexpr int par = decltype(Par)::value, nxt = decltype(NXT)::value, NV = decltype(NVc)::value;
    constexpr bool hqk = decltype(HQK)::value, hpv = decltype(HPV)::value;
    float l = st.l, eprev = 0.f;
    auto soft = [&](auto Rc) AI {
      constexpr int r = decltype(Rc)::value;
      const float e = __builtin_amdgcn_exp2f(st.S[par][r]);
      l += e; PIN(l);
      if constexpr (r & 1) { st.Pk[par][r >> 3][(r >> 1) & 3] = cvtpk(eprev, e); PIN(st.Pk[par][r >> 3]); }
      else { eprev = e; PIN(eprev); }
    };
    sfor<(hqk ? 0 : 8), (hpv ? 16 : 8)>([&](auto Mc) AI {
      constexpr int m = decltype(Mc)::value, n = (!hpv && m >= 4) ? m + 12 : m + 4;
      if constexpr (n < 8) st.F[n & 7] = kfrag(ko, std::integral_constant<int, n>{});
      else if constexpr (n < 16) st.F[n & 7] = vfrag(vo, std::integral_constant<int, n - 8>{});
      else {
        if constexpr (n == 16 && NV >= 0) { bump(aK0, dsl); bump(aK1, dsl); bump(aV, dsl); wait_bar<NV>(); }
        if constexpr (nxt == 1) st.F[n & 7] = kfrag(kn, std::integral_constant<int, n - 16>{});
        if constexpr (nxt == 2) st.F[n & 7] = vfrag(kn, std::integral_constant<int, n - 16>{});
        if constexpr (nxt == 3) st.F[n & 7] = vfrag(kn, std::integral_constant<int, ((n - 16) & 1) * 4 + ((n - 16) >> 1)>{});
      }
      dmaf(Mc);
      if constexpr (m < 8) st.S[par ^ 1] = MFMA(st.F[m & 7], st.Q[m], m == 0 ? st.negm : st.S[par ^ 1]);
      else st.O[m & 3] = MFMA(st.F[m & 7], asfrag(st.Pk[par ^ 1][(m - 8) >> 2]), st.O[m & 3]);
      if constexpr (hqk && hpv) soft(Mc);
      else { soft(std::integral_constant<int, 2 * (m & 7)>{}); soft(std::integral_constant<int, 2 * (m & 7) + 1>{}); }
      SBAR();
    });
    st.l = l;
  };
  using I0 = std::integral_constant<int, 0>; using I1 = std::integral_constant<int, 1>; using IM = std::integral_constant<int, -1>;
  using BT = std::true_type; using BF = std::false_type;
  {
    wait_bar<8>();
    f32x16 s = f32x16{};
    sfor<0, 8>([&](auto I) AI { s = MFMA(kfrag(2 * 8192, I), st.Q[decltype(I)::value], s); });
    sfor<0, 4>([&](auto I) AI { st.F[decltype(I)::value] = kfrag(0, I); });
    float mt = s[0];
#pragma unroll
    for (int r = 1; r < 16; ++r) mt = __builtin_fmaxf(mt, s[r]);
    mt = swap_max(mt) + M_MARGIN;
#pragma unroll
    for (int r = 0; r < 16; ++r) { st.S[0][r] = s[r] - mt; st.negm[r] = -mt; }
    PIN(st.negm);
    st.l = 0.f;
    SBAR();
  }
  auto nodma = [&](auto) AI {};
  auto warmF = [&](auto Uc) AI {
    constexpr int u = decltype(Uc)::value;
    if constexpr (u < 12) { if (w == (u & 7)) {
        unsigned t = lane; PIN(t); t += (u % 3) * 64;
        if constexpr (u < 3) touch(lds0 + A_JUNK, (256 * j + 16 * rb) * 1152 + (t < 144 ? t : 0) * 128, rWo);
        else if constexpr (u < 9) { const unsigned rr = t / 5, cc = t - rr * 5;
          touch(lds0 + A_JUNK, (36 * rb + (rr < 36 ? rr : 0)) * (F_ * 2) + ((576 * j) & ~127u) + cc * 128, mkrsrc(u < 6 ? wg : wu, D_ * F_ * 2)); }
        else touch(lds0 + A_JUNK, (288 * j + 18 * rb) * 1152 + (t < 162 ? t : 0) * 128, mkrsrc(wd, F_ * D_ * 2));
    } }
  };
  sfor<0, 8>([&](auto Ic) AI {
    constexpr int i = decltype(Ic)::value, C = 9 + i, dsl = (((C + 1) & 3) - (C & 3)) * A_SLOT;
    auto dmaf = [&](auto Mc) AI { constexpr int m = decltype(Mc)::value;
      if constexpr (i == 0) { if constexpr (m & 1) issue(std::integral_constant<int, 12>{}, std::integral_constant<int, (m >> 1)>{}); }
      else if constexpr (i < 7 && (m & 3) == 1) issue(std::integral_constant<int, C + 3>{}, std::integral_constant<int, (m >> 2)>{});
      if constexpr (i > 0 && m == 15) warmF(std::integral_constant<int, 3 * i - 1>{}); };
    if constexpr (i == 0) step(I0{}, BT{}, BF{}, I1{}, IM{}, 0, 0, 0, 8192, 0, dmaf);
    else if constexpr (i < 7) step(I0{}, BT{}, BT{}, I1{}, IM{}, 2 * i, 0, 2 * 8192, 8192, 0, dmaf);
    else step(I0{}, BT{}, BT{}, std::integral_constant<int, 2>{}, IM{}, 2 * i, 0, 2 * 8192, 3 * 8192, 0, dmaf);
    auto warm = [&](auto Mc) AI { constexpr int m = decltype(Mc)::value;
      if constexpr (m == 4) warmF(std::integral_constant<int, 3 * i>{});
      if constexpr (m == 10) warmF(std::integral_constant<int, 3 * i + 1>{}); };
    if constexpr (i < 7) step(I1{}, BT{}, BT{}, I1{}, std::integral_constant<int, 8>{}, 2 * i + 1, 8192, 3 * 8192, 0, dsl, warm);
    else step(I1{}, BF{}, BT{}, std::integral_constant<int, 3>{}, IM{}, 15, 0, 3 * 8192, 8192, 0, nodma);
  });
  bf16x8 Of[16];
  {
    sfor<0, 8>([&](auto Ic) AI { constexpr int i = decltype(Ic)::value;
      if constexpr (i < 4) st.F[4 + i] = vfrag(8192, std::integral_constant<int, (i & 1) * 4 + 2 + (i >> 1)>{});
      st.O[i >> 1] = MFMA(st.F[i], asfrag(st.Pk[1][i & 1]), st.O[i >> 1]); SBAR(); });
    STAMP(3);
    if (__builtin_expect(__builtin_amdgcn_ballot_w64(!(st.l <= L_LIMIT)) != 0, 0)) {
      const u16* kg = kc + ((size_t)j * SK + (SK - WIN) + r32) * DH + 8 * h;
      const u16* vg = vc + ((size_t)j * SK + (SK - WIN) + 4 * h) * DH + r32;
      float mt = -__builtin_inff();
#pragma unroll 1
      for (int u = 0; u < 16; ++u) {
        f32x16 s = f32x16{};
#pragma unroll
        for (int i = 0; i < 8; ++i) s = MFMA(*(const bf16x8*)(kg + (size_t)u * 32 * DH + 16 * i), st.Q[i], s);
#pragma unroll
        for (int r = 0; r < 16; ++r) mt = __builtin_fmaxf(mt, s[r]);
      }
      mt = swap_max(mt);
      float l = 0.f;
#pragma unroll
      for (int t = 0; t < 4; ++t) st.O[t] = f32x16{};
#pragma unroll 1
      for (int u = 0; u < 16; ++u) {
        f32x16 s = f32x16{};
#pragma unroll
        for (int i = 0; i < 8; ++i) s = MFMA(*(const bf16x8*)(kg + (size_t)u * 32 * DH + 16 * i), st.Q[i], s);
#pragma unroll
        for (int r = 0; r < 16; ++r) { s[r] = __builtin_amdgcn_exp2f(s[r] - mt); l += s[r]; }
#pragma unroll
        for (int ks = 0; ks < 2; ++ks) {
          const bf16x8 pf = asfrag(pack8(s, ks));
#pragma unroll
          for (int t = 0; t < 4; ++t) {
            bf16x8 vf;
#pragma unroll
            for (int e = 0; e < 8; ++e) vf[e] = (short)vg[(size_t)(u * 32 + 16 * ks + 8 * (e >> 2) + (e & 3)) * DH + 32 * t];
            st.O[t] = MFMA(vf, pf, st.O[t]);
          }
        }
      }
      st.l = l;
    }
    const float inv = __builtin_amdgcn_rcpf(swap_add(st.l));
    const lptr xw = L + (hd ? aslot(15) : A_SPARE) + g * 8192 + lane * 16, xr = L + (hd ? A_SPARE : aslot(15)) + g * 8192 + lane * 16;
#pragma unroll
    for (int t = 0; t < 4; ++t) {
#pragma unroll
      for (int r = 0; r < 16; ++r) st.O[t][r] *= inv;
      Of[2 * t] = asfrag(pack8(st.O[t], 0)); Of[2 * t + 1] = asfrag(pack8(st.O[t], 1));
      *(LAS bf16x8*)(xw + 2 * t * 1024) = Of[2 * t]; *(LAS bf16x8*)(xw + (2 * t + 1) * 1024) = Of[2 * t + 1];
    }
    wait_bar<4>();
#pragma unroll
    for (int i = 0; i < 8; ++i) Of[8 + i] = ldfrag(xr + i * 1024);
    SBAR();
  }
  STAMP(4);
  const unsigned hf = hd;
  const rsrc_t rGU = mkrsrc(hf ? wu : wg, D_ * F_ * 2), rWd = mkrsrc(wd, F_ * D_ * 2), rH2 = mkrsrc(ws + WS_H2, S_ * D_ * 2);
  unsigned lane2 = lane; PIN(lane2);
  unsigned voffGU[5];
  auto setGU = [&](auto NSc, auto Pc, unsigned ln) AI {
    constexpr int NS = decltype(NSc)::value, p = decltype(Pc)::value;
    const unsigned r8v = (ln >> 2) & 7;
    const unsigned piece = p < 4 ? g + 4 * p : 16 + (g & 1), stl = 2 * piece + (ln >> 5), R = stl / NS, sub = stl - NS * R;
    voffGU[p] = (8 * R + r8v) * (F_ * 2) + 576 * j + sub * 64 + (((ln & 3) ^ (GU32 ? hf * 2 : (r8v >> 2) * 2)) * 16);
    PIN(voffGU[p]);
  };
  sfor<0, 5>([&](auto P) AI { setGU(std::integral_constant<int, 9>{}, P, lane2); });
  const unsigned m0gu = lds0 + hf * 18432 + g * 1024, m0gux = lds0 + hf * 18432 + (16 + (g & 1)) * 1024;
  const bool xgu = (g >> 1) != 0;
  unsigned voffWd = 0;
  auto issueF = [&](auto Cc, auto Pc) AI {
    constexpr int C = decltype(Cc)::value, P = decltype(Pc)::value, slot = bslot(C);
    if constexpr (C < 18) {
      constexpr unsigned so = C < 12 ? C * 32 * F_ * 2 : (384 + 96 * (C & 1)) * F_ * 2 + 192 * ((C - 12) >> 1);
      if constexpr (P < 4) dma16<0>(m0gu + slot + P * 4096, voffGU[P], rGU, so);
      else { if (xgu == (bool)(C & 1)) dma16<0>(m0gux + slot, voffGU[4], rGU, so); }
    } else if constexpr (C < 27) {
      if constexpr (P < 4) dma16<0>(m0w1 + slot + P * 8192, voffWd, rWd, (C - 18) * 128 + P * 64 * 1152);
      else { if (xgu == (bool)(C & 1)) {
          const unsigned e = (g & 1) + 2 * hf;
          dma16<0>(lds0 + (32 + e) * 1024 + slot, voffWd, rWd, (C - 18) * 128 + (GU32 ? (32 + e - w) * 8 : 256 + 16 * hf + 4 * (g & 1) - 16 * (w >> 1) - 4 * (w & 1)) * 1152); } }
    }
  };
  const rsrc_t rP = mkrsrc(ws + WS_P, 16 * ACT_B);
  auto project = [&](auto NKc, auto CBc, const bf16x8* Bf, unsigned aOwn, unsigned aPar, auto&& dslot, auto&& nv, auto&& dma, auto&& preload, auto&& extra, auto&& early, float (&y)[10]) AI {
    constexpr int NK = decltype(NKc)::value, HK = NK / 2, CB = decltype(CBc)::value, NA = XNA;
    unsigned ln = lane; PIN(ln);
    const unsigned cons = 4 * g + ((ln & 31) >> 3);
    const unsigned voffP = rb * 16 * 147456 + (CB == 0 ? cons * 147456 + j * 128 : j * 147456 + cons * 128) + (4 * hd + (ln >> 5)) * 2048 + (ln & 7) * 16;
    const unsigned vo = rb * 16 * 147456 + w * 2048 + (CB == 0 ? j * 147456 + ln * 16 : (ln >> 3) * 147456 + j * 128 + (ln & 7) * 16);
    const unsigned co = rb * 128 + CB * 2048;
    PIN(aOwn); PIN(aPar);
    bf16x8 F[8]; f32x16 acc[2]; u32x4 pv[18]; u32x2 pl;
    const unsigned vf = (ln & 15) * 8;
    auto ldp = [&](auto Tc) AI { constexpr int t = decltype(Tc)::value; gld16c<0>(pv[t], vo, rP, (t >> 1) * 16384 + (t & 1) * (CB == 0 ? 1024 : 8 * 147456)); };
    f32x4 D[5]; u32x4 sel[2];
    auto mksel = [&]() AI {
#pragma unroll
      for (int d = 0; d < 4; ++d) {
        const unsigned v = (ln & 7) == 2 * d ? 0x3f80u : (ln & 7) == 2 * d + 1 ? 0x3f800000u : 0u;
        sel[0][d] = (ln & 8) ? 0u : v; sel[1][d] = (ln & 8) ? v : 0u;
      }
    };
    auto red = [&](auto Tc) AI {
      constexpr int t = decltype(Tc)::value;
      f32x4 c; if constexpr (t & 1) c = D[t >> 1]; else c = f32x4{};
      c = __builtin_amdgcn_mfma_f32_16x16x32_bf16(asfrag(sel[t & 1]), asfrag(pv[2 * t]), c, 0, 0, 0);
      D[t >> 1] = __builtin_amdgcn_mfma_f32_16x16x32_bf16(asfrag(sel[t & 1]), asfrag(pv[2 * t + 1]), c, 0, 0, 0);
      if constexpr ((t & 1) || t == 8) {
        const f32x4 d = D[t >> 1];
        const float v0 = d[0] + ror8(d[0]), v1 = d[1] + ror8(d[1]), v2 = d[2] + ror8(d[2]), v3 = d[3] + ror8(d[3]);
        y[t & ~1] = (ln & 8) ? v2 : v0; y[t | 1] = (ln & 8) ? v3 : v1;
      }
    };
    auto stage = [&](auto Nc) AI {
      constexpr int n = decltype(Nc)::value, i = n / NK, ks = n % NK;
      if constexpr (ks == 0 && i > 0) {
        bump(aOwn, dslot(i)); bump(aPar, dslot(i));
        constexpr int NV = decltype(nv(std::integral_constant<int, i>{}))::value;
        if constexpr (NA > 0 && i == NA + 5) {
          asm volatile("s_waitcnt vmcnt(%1)" : "+v"(pl) : "i"(NV) : "memory");
          poll_until(pl, vf, rC, co, tag, NA, j * 8);
          asm volatile("s_barrier" ::: "memory");
        } else asm volatile("s_waitcnt vmcnt(%0)\n\ts_barrier" :: "i"(NV) : "memory");
        if constexpr ((NA > 0 && i == NA + 2) || i == 8) signal(rC, co + j * 8, tag, i - 2, tid);
        if constexpr (NA > 0 && i == NA + 4) gld8c(pl, vf, rC, co);
      }
      const lptr b = lp(ks < HK ? aOwn : aPar) + (CB == 0 ? ks % HK : 6 * ((ks % HK) / 3) + ks % 3) * 2048;
      F[n & 7] = trfrag(b, b + 1024);
      if constexpr (NA > 0 && i == NA + 5 && ks < 2 * NA) ldp(std::integral_constant<int, ks>{});
      dma(std::integral_constant<int, i>{}, std::integral_constant<int, ks>{});
    };
    sfor<0, 4>([&](auto N) AI { stage(N); SBAR(); });
    sfor<0, 9 * NK>([&](auto Mc) AI {
      constexpr int m = decltype(Mc)::value, i = m / NK, ks = m % NK;
      if constexpr (m + 4 < 9 * NK) stage(std::integral_constant<int, m + 4>{});
      if constexpr (ks == 0) acc[i & 1] = MFMA(F[m & 7], Bf[0], f32x16{});
      else acc[i & 1] = MFMA(F[m & 7], Bf[ks], acc[i & 1]);
      if constexpr (i > 0 && (ks == 3 || ks == 5)) { constexpr int s = (ks - 3) >> 1;
        __builtin_amdgcn_raw_buffer_store_b128(pack8(acc[(i - 1) & 1], s), rP, voffP, (i - 1) * 16384 + s * 4096, 16); }
      SBAR();
    });
    __builtin_amdgcn_raw_buffer_store_b128(pack8(acc[0], 0), rP, voffP, 8 * 16384, 16);
    __builtin_amdgcn_raw_buffer_store_b128(pack8(acc[0], 1), rP, voffP, 8 * 16384 + 4096, 16);
    STAMP(CB == 0 ? 5 : 12);
    gld8c(pl, vf, rC, co);
    asm volatile("s_waitcnt vmcnt(0) lgkmcnt(0)\n\ts_barrier" : "+v"(pl) :: "memory");
    if constexpr (CB == 0) STAMP(30);
    signal(rC, co + j * 8, tag, 9, tid);
#pragma unroll
    for (int t = 0; t < 2 * NA; ++t) PIN(pv[t]);
    poll_until(pl, vf, rC, co, tag, 6, j * 8);
    sfor<2 * NA, 12>(ldp);
    extra();
    mksel();
    sfor<0, NA>(red);
    preload();
    wait_group(w, vf, rC, co, tag, 9, j * 8);
    STAMP(CB == 0 ? 6 : 13);
    sfor<12, 18>(ldp);
    asm volatile("s_waitcnt vmcnt(6)" ::: "memory");
#pragma unroll
    for (int t = 2 * NA; t < 12; ++t) PIN(pv[t]);
    sfor<NA, 6>(red);
    early();
    sfor<6, 9>([&](auto Tc) AI {
      constexpr int t = decltype(Tc)::value;
      asm volatile("s_waitcnt vmcnt(%2)" : "+v"(pv[2 * t]), "+v"(pv[2 * t + 1]) : "i"(16 - 2 * t) : "memory");
      red(Tc);
    });
  };
  auto rowsum = [&](float v, int slot) AI {
    v += ror8(v); v = swap16_add(v, v); v = swap32_add(v, v);
    if (lane < 8) *(LAS float*)(L + SP_RED + slot * 256 + lane * 32 + w * 4) = v;
    lbar();
    const f32x4 r0 = *(LAS f32x4*)(L + SP_RED + slot * 256 + (lane & 7) * 32), r1 = *(LAS f32x4*)(L + SP_RED + slot * 256 + (lane & 7) * 32 + 16);
    return ((r0[0] + r0[1]) + (r0[2] + r0[3])) + ((r1[0] + r1[1]) + (r1[2] + r1[3]));
  };
  {
    float x2[10], y[10], cf[10], sf[10], m0, s1, s2; unsigned xp[5], ga[5], sb[5], cb[5]; u32x4 tbv;
    const int e = lane >> 3, row8 = lane & 7;
    const lptr xk = L + SP_X + row8 * 1152 + ncol(w, lane) * 2;
    const unsigned aOwn = lds0 + (17 & 3) * A_SLOT + trb + cp * 8 + hd * 512 + hd * 16384;
    project(std::integral_constant<int, 16>{}, I0{}, Of, aOwn, aOwn ^ 16384,
            [](int i) AI { return aslot(17 + i) - aslot(16 + i); },
            [](auto Ic) AI { constexpr int i = decltype(Ic)::value;
              constexpr int ops[9] = {4, 6, 6, 6, 6, 6, 8, 2, 2}, NA = XNA;
              return std::integral_constant<int, ops[i - 1] + (NA > 0 && i - 1 == NA + 5 ? 2 * NA : 0)>{}; },
            [&](auto Ic, auto Kc) AI {
              constexpr int C = 17 + decltype(Ic)::value, ks = decltype(Kc)::value;
              if constexpr (C < 24 && (ks & 3) == 1) issue(std::integral_constant<int, C + 2>{}, std::integral_constant<int, (ks >> 2)>{});
              if constexpr (C == 23) {
                if constexpr (ks == 3) dma16<0>(m0w1 + SP_X, lane2 * 16 + w * 1024, rXr, (rb * 128 + j * 8) * 1152);
                if constexpr (ks == 11) gld16c<0>(tbv, (w < 5 ? w * 64 + lane2 : 0) * 16, rC, WS_TABB);
                if constexpr (ks == 15) { if (w == 0) dma16<0>(lds0 + SP_X + 8192, lane2 * 16, rXr, (rb * 128 + j * 8) * 1152 + 8192); }
              }
              if constexpr (C < 25) {}
              else if constexpr (ks == 8) {
                PIN(tbv);
                if (w < 5) *(LAS u32x4*)(L + B_SPARE + (w * 64 + lane2) * 16) = tbv;
              }
            },
            [&]() AI {
              const lptr tb = L + B_SPARE + ncol(w, lane2) * 2, xr = L + SP_X + (lane2 & 7) * 1152 + ncol(w, lane2) * 2;
#pragma unroll
              for (int i = 0; i < 5; ++i) {
                xp[i] = *(LAS unsigned*)(xr + i * 256); ga[i] = *(LAS unsigned*)(tb + i * 256);
                sb[i] = *(LAS unsigned*)(tb + 1152 + i * 256); cb[i] = *(LAS unsigned*)(tb + 2 * 1152 + i * 256);
                PIN(xp[i]); PIN(ga[i]); PIN(sb[i]); PIN(cb[i]);
              }
            },
            [&]() AI { sfor<0, 2>([&](auto C) AI { sfor<0, 5>([&](auto P) AI { issueF(C, P); }); }); },
            [&]() AI {
              float s = 0.f;
#pragma unroll
              for (int i = 0; i < 3; ++i) {
                x2[2 * i] = bflo(xp[i]) + bflo(ga[i]) * y[2 * i]; x2[2 * i + 1] = bfhi(xp[i]) + bfhi(ga[i]) * y[2 * i + 1];
                s += x2[2 * i] + x2[2 * i + 1];
                *(LAS unsigned*)(xk + i * 256) = cvtpk(x2[2 * i], x2[2 * i + 1]);
              }
              m0 = rowsum(s, 0) * (1.f / 384);
              s1 = 0.f; s2 = 0.f;
#pragma unroll
              for (int i = 0; i < 6; ++i) { x2[i] -= m0; s1 += x2[i]; s2 += x2[i] * x2[i]; PIN(x2[i]); }
#pragma unroll
              for (int i = 0; i < 5; ++i) {
                cf[2 * i] = bflo(cb[i]) * LOG2E; cf[2 * i + 1] = bfhi(cb[i]) * LOG2E; sf[2 * i] = bflo(sb[i]) * LOG2E; sf[2 * i + 1] = bfhi(sb[i]) * LOG2E;
                PIN(cf[2 * i]); PIN(cf[2 * i + 1]); PIN(sf[2 * i]); PIN(sf[2 * i + 1]);
              }
              PIN(s1); PIN(s2); PIN(m0);
            }, y);
    const bool tail = lane < 32;
#pragma unroll
    for (int i = 3; i < 5; ++i) {
      x2[2 * i] = bflo(xp[i]) + bflo(ga[i]) * y[2 * i]; x2[2 * i + 1] = bfhi(xp[i]) + bfhi(ga[i]) * y[2 * i + 1];
      xp[i] = cvtpk(x2[2 * i], x2[2 * i + 1]);
      x2[2 * i] -= m0; x2[2 * i + 1] -= m0;
      const float t1 = x2[2 * i] + x2[2 * i + 1], t2 = x2[2 * i] * x2[2 * i] + x2[2 * i + 1] * x2[2 * i + 1];
      s1 += (i < 4 || tail) ? t1 : 0.f; s2 += (i < 4 || tail) ? t2 : 0.f;
    }
    float sv = swap32_add(s1, s2); sv += ror8(sv); sv = swap16_add(sv, sv);
    if ((lane & 24) == 0) *(LAS float*)(L + SP_RED + 256 + row8 * 64 + (lane >> 5) * 32 + w * 4) = sv;
    lbar();
    const lptr rp = L + SP_RED + 256 + row8 * 64;
    const f32x4 r0 = *(LAS f32x4*)rp, r1 = *(LAS f32x4*)(rp + 16), r2 = *(LAS f32x4*)(rp + 32), r3 = *(LAS f32x4*)(rp + 48);
    *(LAS unsigned*)(xk + 3 * 256) = xp[3];
    if (tail) *(LAS unsigned*)(xk + 4 * 256) = xp[4];
    const float dm = (((r0[0] + r0[1]) + (r0[2] + r0[3])) + ((r1[0] + r1[1]) + (r1[2] + r1[3]))) * (1.f / D_);
    const float var = (((r2[0] + r2[1]) + (r2[2] + r2[3])) + ((r3[0] + r3[1]) + (r3[2] + r3[3]))) * (1.f / D_) - dm * dm;
    const float rstd = __builtin_amdgcn_rsqf(__builtin_fmaxf(var, 0.f) + LN_EPS);
    const lptr sc = L + bslot(3) + w * 1280;
#pragma unroll
    for (int i = 0; i < 5; ++i)
      *(LAS unsigned*)(sc + i * 256 + (lane >> 5) * 128 + row8 * 16 + (e & 3) * 4) =
          cvtpk((x2[2 * i] - dm) * rstd * cf[2 * i] + sf[2 * i], (x2[2 * i + 1] - dm) * rstd * cf[2 * i + 1] + sf[2 * i + 1]);
    asm volatile("s_waitcnt lgkmcnt(0)" ::: "memory");
    const u32x4 c0 = *(LAS u32x4*)(sc + lane * 16), c1 = *(LAS u32x4*)(sc + 1024 + row8 * 16);
    const unsigned ho = (rb * 4 + (j >> 2)) * 36864 + (GU32 ? w * 512 + ((j & 3) * 8 + row8) * 16 : (w >> 2) * 2048 + ((j >> 1) & 1) * 1024 + (w & 3) * 256 + ((j & 1) * 8 + row8) * 16);
    __builtin_amdgcn_raw_buffer_store_b128(c0, rH2, ho + e * 4096, 0, 16);
    if (lane < 8) __builtin_amdgcn_raw_buffer_store_b128(c1, rH2, ho + 8 * 4096, 0, 16);
    STAMP(7);
    if (blockIdx.x == 0 && w < 4) poll_until(u32x2{0u, ~tag[1]}, lane * 8, rC, 7 * 2048 + w * 512, tag, 1, ~0u);
    asm volatile("s_waitcnt vmcnt(0)\n\ts_barrier" ::: "memory");
    signal(rC, 3 * 2048 + rb * 128 + j * 8, tag, 1, tid);
    if (blockIdx.x == 0) signal(rC, WS_EPOCH, tag, 0, tid);
    wait_group(w, (lane & 15) * 8, rC, 3 * 2048 + rb * 128, tag, 1, j * 8);
    STAMP(8);
  }
  bf16x8 F[8], Af[18];
  unsigned lane3 = lane; PIN(lane3);
  const int trb3 = (lane3 >> 5) * 256 + ((lane3 >> 2) & 3) * 64 + (lane3 & 3) * 8, blk3 = (lane3 >> 4) & 1;
  {
    const unsigned voffH = lane3 * 16 + rg * (NKS * 1024);
    u32x4 H[NKS];
    auto issueH = [&](auto Ic) AI { constexpr int i = decltype(Ic)::value;
      gld16c<1024 * (i & 3)>(H[i], voffH, rH2, (i >> 2) * 4096);
    };
    sfor<0, 6>(issueH);
    const unsigned lb = lds0 + bslot(0) + (GU32 ? trb3 + blk3 * 18432 : (blk3 * 4 + ((lane3 >> 2) & 3)) * 64 + (lane3 & 3) * 8 + (lane3 >> 5) * 9216);
    unsigned aE = lb + hf * 512 + ((hf ^ blk3) * 32), aO = lb + hf * 1024 + ((1 ^ hf ^ blk3) * 32);
    PIN(aE); PIN(aO);
    { const unsigned r8v = (lane3 >> 2) & 7;
      voffWd = (288 * j + (GU32 ? 8 * w + r8v : 16 * (w >> 1) + 4 * (w & 1) + 8 * (r8v >> 2) + (r8v & 3))) * 1152 + (lane3 >> 5) * 64 + (lane3 & 3) * 16; }
    f32x4 acc[9][2][2];
    f32x16 acc32[9];
    struct GU { static constexpr int tile(int n) { return n < 216 ? (GU32 ? n % 9 : (n % 18) >> 1) : 3 * ((n / 18 - 12) >> 1) + (n % 18) / 6; }
                static constexpr int kstep(int n) { return n < 216 ? n / 18 : 12 + 3 * ((n / 18) & 1) + ((n % 6) >> 1); }
                static constexpr int k16(int n) { return n < 216 ? n / 9 : 24 + 6 * ((n / 18) & 1) + n % 6; }
                static constexpr int hops(int p) { return p < 9 ? 2 : p < 12 ? 4 : 0; } };
    auto stage = [&](auto Nc) AI {
      constexpr int n = decltype(Nc)::value, C = n / 18, r = n % 18, T = GU::tile(n), mm = n & 1;
      if constexpr (r == 0) {
        if constexpr (C > 0) {
          unsigned d = bslot(C) - bslot(C - 1);
          if constexpr (C == 12 && !GU32) d -= (lane3 >> 5) * 6144;
          aE += d; aO += d; PIN(aE); PIN(aO);
        }
        constexpr int NV = C == 0 ? 4 : C == 1 ? 8 : 4 + GU::hops(C - 1);
        if constexpr (C < 12) asm volatile("s_waitcnt vmcnt(%2)\n\ts_barrier" : "+v"(H[2 * C]), "+v"(H[2 * C + 1]) : "i"(NV) : "memory");
        else if constexpr (C < 14) { constexpr int b = 24 + 6 * (C - 12);
          asm volatile("s_waitcnt vmcnt(%6)\n\ts_barrier" : "+v"(H[b]), "+v"(H[b + 1]), "+v"(H[b + 2]), "+v"(H[b + 3]), "+v"(H[b + 4]), "+v"(H[b + 5]) : "i"(NV) : "memory"); }
        else asm volatile("s_waitcnt vmcnt(%0)\n\ts_barrier" :: "i"(NV) : "memory");
      }
#define PRIO_AT 12
      if constexpr (r == 0) prio_up(hf);
      if constexpr (r == PRIO_AT) __builtin_amdgcn_s_setprio(0);
      constexpr int off = GU32 ? (C < 12 ? 3 * (T / 3) + (T % 3) / 2 + 18 * (r / 9) : (T % 3) / 2 + 6 * (n % 6)) * 512
                               : mm * 18432 + (C < 12 ? 3 * (T / 3) + (T % 3) / 2 : (T % 3) / 2 + 12 * ((n % 6) >> 1)) * 512;
      const lptr b = lp(T % 3 == 1 ? aO : aE) + off;
      F[n & 7] = trfrag(b, b + (C < 12 ? 9 : 3) * 512);
      if constexpr (r % 4 == 0 && r / 4 < GU::hops(C)) issueH(std::integral_constant<int, (C < 9 ? 2 * C + 6 : 4 * C - 12) + r / 4>{});
      if constexpr (r % 4 == 2 || r == 16) issueF(std::integral_constant<int, C + 2>{}, std::integral_constant<int, r / 4>{});
      if constexpr (C == 9 && (r % 4 == 3 || r == 17)) { unsigned ln = lane; PIN(ln); setGU(std::integral_constant<int, 3>{}, std::integral_constant<int, r / 4>{}, ln); }
    };
    float e0, e1; unsigned pk[4];
    auto silu = [&](auto Gc, auto Sc) AI {
      constexpr int S = decltype(Sc)::value, u = S / 3, part = S % 3, T = 3 * decltype(Gc)::value + u / 4, rt = (u >> 1) & 1, d = u & 1, q = 2 * (u & 3);
      const float g0 = GU32 ? acc32[T][q] : acc[T][0][rt][2 * d], g1 = GU32 ? acc32[T][q + 1] : acc[T][0][rt][2 * d + 1];
      if constexpr (part == 0) {
        e0 = __builtin_amdgcn_exp2f(-g0); e1 = __builtin_amdgcn_exp2f(-g1);
        PIN(e0); PIN(e1);
      } else if constexpr (part == 1) {
        e0 = __builtin_amdgcn_rcpf(1.f + e0); e1 = __builtin_amdgcn_rcpf(1.f + e1);
        PIN(e0); PIN(e1);
      } else {
        const float u0 = GU32 ? acc32[T][8 + q] : acc[T][1][rt][2 * d], u1 = GU32 ? acc32[T][9 + q] : acc[T][1][rt][2 * d + 1];
        pk[u & 3] = cvtpk(g0 * u0 * e0, g1 * u1 * e1); PIN(pk[u & 3]);
        if constexpr ((u & 3) == 3) { Af[T] = GU32 ? asfrag(u32x4{pk[0], pk[1], pk[2], pk[3]}) : join16(pk[0], pk[1], pk[2], pk[3]); PIN(Af[T]); }
      }
    };
#define GPD 4
    sfor<0, GPD>([&](auto N) AI { stage(N); SBAR(); });
    STAMP(9);
    sfor<0, 324>([&](auto Mc) AI {
      constexpr int m = decltype(Mc)::value, T = GU::tile(m), ks = GU::kstep(m), mm = m & 1;
      if constexpr (GU32) { if constexpr (m < 9) acc32[T] = MFMA(F[m & 7], asfrag(H[GU::k16(m)]), f32x16{}); else acc32[T] = MFMA(F[m & 7], asfrag(H[GU::k16(m)]), acc32[T]); }
      else mfma16<(m < 18)>(acc[T][mm][0], F[m & 7], H[2 * ks]);
      if constexpr (m + GPD < 324) stage(std::integral_constant<int, m + GPD>{});
      if constexpr (m >= 241 && m < 313) silu(std::integral_constant<int, (m - 241) / 36>{}, std::integral_constant<int, (m - 241) % 36>{});
      if constexpr (m >= 313) silu(std::integral_constant<int, 2>{}, std::integral_constant<int, 2 * (m - 313)>{});
      if constexpr (!GU32) { SBAR(); mfma16<(m < 18)>(acc[T][mm][1], F[m & 7], H[2 * ks + 1]); }
      if constexpr (m >= 313) silu(std::integral_constant<int, 2>{}, std::integral_constant<int, 2 * (m - 313) + 1>{});
      SBAR();
    });
    STAMP(10);
    asm volatile("s_nop 7"); SBAR();
    lbar();
    const lptr xw = L + bslot(16) + w * 9216 + lane3 * 16, xr = L + bslot(16) + (w ^ 4) * 9216 + lane3 * 16;
#pragma unroll
    for (int t = 0; t < 7; ++t) *(LAS bf16x8*)(xw + t * 1024) = Af[t];
    sfor<22, 36>([&](auto Sc) AI { silu(std::integral_constant<int, 2>{}, Sc); });
    *(LAS bf16x8*)(xw + 7 * 1024) = Af[7]; *(LAS bf16x8*)(xw + 8 * 1024) = Af[8];
    wait_bar<4>();
#pragma unroll
    for (int t = 0; t < 9; ++t) Af[9 + t] = ldfrag(xr + t * 1024);
    SBAR();
  }
  STAMP(11);
  {
    float y[10]; unsigned gb[5], xp[5];
    const lptr im = L + (lane3 & 7) * 1152 + ncol(w, lane3) * 2;
    constexpr float GS = 1.f / (LOG2E * LOG2E);
    const unsigned aT = lds0 + bslot(18) + trb3 + blk3 * 32 + hf * 512;
    project(std::integral_constant<int, 18>{}, std::integral_constant<int, 4>{}, Af, aT + hf * 6144, aT + (1 - hf) * 6144,
            [](int i) AI { return bslot(18 + i) - bslot(17 + i); },
            [](auto Ic) AI { constexpr int i = decltype(Ic)::value;
              constexpr int ops[9] = {4, 6, 6, 6, 6, 6, 6, 2, 2}, NA = XNA;
              return std::integral_constant<int, ops[i - 1] + (NA > 0 && i - 1 == NA + 5 ? 2 * NA : 0)>{}; },
            [&](auto Ic, auto Kc) AI {
              constexpr int C = 18 + decltype(Ic)::value, ks = decltype(Kc)::value;
              if constexpr (C == 18) {
                if constexpr (ks == 8) asm volatile("s_barrier" ::: "memory");
                if constexpr (ks >= 9 && (ks & 1)) issueF(std::integral_constant<int, 20>{}, std::integral_constant<int, (ks - 9) / 2>{});
              } else if constexpr (ks >= 1 && ks < 16 && (ks % 3) == 1) issueF(std::integral_constant<int, C + 2>{}, std::integral_constant<int, (ks / 3)>{});
            },
            [&]() AI {
#pragma unroll
              for (int i = 0; i < 5; ++i) {
                gb[i] = *(LAS unsigned*)(L + B_SPARE + 3 * 1152 + ncol(w, lane3) * 2 + i * 256);
                xp[i] = *(LAS unsigned*)(L + SP_X + (lane3 & 7) * 1152 + ncol(w, lane3) * 2 + i * 256);
                PIN(gb[i]); PIN(xp[i]);
              }
            },
            [&]() AI {},
            [&]() AI {
#pragma unroll
              for (int i = 0; i < 3; ++i) *(LAS unsigned*)(im + i * 256) = cvtpk(bflo(xp[i]) + bflo(gb[i]) * GS * y[2 * i], bfhi(xp[i]) + bfhi(gb[i]) * GS * y[2 * i + 1]);
            }, y);
    unsigned ln = lane; PIN(ln);
#pragma unroll
    for (int i = 3; i < 5; ++i)
      if (i < 4 || ln < 32) *(LAS unsigned*)(im + i * 256) = cvtpk(bflo(xp[i]) + bflo(gb[i]) * GS * y[2 * i], bfhi(xp[i]) + bfhi(gb[i]) * GS * y[2 * i + 1]);
    lbar();
    u16* op = out + (size_t)(rb * 128 + j * 8) * D_;
    const unsigned t2 = w * 64 + ln;
#define OUT_AUX 16
    const rsrc_t rO = mkrsrc(op, 9216);
    __builtin_amdgcn_raw_buffer_store_b128(*(LAS u32x4*)(L + t2 * 16), rO, t2 * 16, 0, OUT_AUX);
    if (w == 0) __builtin_amdgcn_raw_buffer_store_b128(*(LAS u32x4*)(L + 8192 + ln * 16), rO, 8192 + ln * 16, 0, OUT_AUX);
  }
  asm volatile(".Lkm_end:");
  STAMP(14);
}

extern "C" void kernel_launch(void* const* d_in, const int* in_sizes, int n_in, void* d_out, int out_size, void* d_ws, size_t ws_size,
                              hipStream_t stream) {
  (void)in_sizes; (void)n_in; (void)out_size; (void)ws_size;
  const u16* x = (const u16*)d_in[0]; const u16* kc = (const u16*)d_in[1]; const u16* vc = (const u16*)d_in[2];
  const u16* wq = (const u16*)d_in[3]; const u16* wo = (const u16*)d_in[4];
  const u16* wg = (const u16*)d_in[6]; const u16* wu = (const u16*)d_in[7]; const u16* wd = (const u16*)d_in[8];
  const u16* c = (const u16*)d_in[9]; const u16* wmod = (const u16*)d_in[10];
  char* ws = (char*)d_ws;
  (void)hipFuncSetAttribute((const void*)kmain, hipFuncAttributeMaxDynamicSharedMemorySize, A_LDS);
  kmain<<<256, 512, A_LDS, stream>>>(x, wq, kc, vc, wo, wg, wu, wd, c, wmod, ws, (u16*)d_out);
}
```

```cpp
#include <hip/hip_runtime.h>
#include <stdint.h>
#include <type_traits>

typedef short    bf16x8 __attribute__((ext_vector_type(8)));
typedef short    s16x4  __attribute__((ext_vector_type(4)));
typedef float    f32x16 __attribute__((ext_vector_type(16)));
typedef float    f32x4  __attribute__((ext_vector_type(4)));
typedef float    f32x2  __attribute__((ext_vector_type(2)));
typedef __bf16   bf16x2 __attribute__((ext_vector_type(2)));
typedef unsigned u32x4  __attribute__((ext_vector_type(4)));
typedef unsigned u32x2  __attribute__((ext_vector_type(2)));
typedef unsigned short u16;
#define LAS __attribute__((address_space(3)))
typedef LAS char* lptr;
typedef __amdgpu_buffer_rsrc_t rsrc_t;
#define DI __device__ __forceinline__
#define AI __attribute__((always_inline))
#define SBAR() __builtin_amdgcn_sched_barrier(0)
#define PIN(x) asm volatile("" : "+v"(x))
#define MFMA(a, b, c) __builtin_amdgcn_mfma_f32_32x32x16_bf16((a), (b), (c), 0, 0, 0)

constexpr int S_ = 2048, D_ = 576, HQ = 32, HKV = 16, SK = 2048, DH = 128, WIN = 512, F_ = 4608;
constexpr int NKS = D_ / 16;
constexpr int NRG = S_ / 32;
constexpr float LN_EPS = 1e-5f, LOG2E = 1.4426950408889634f;
constexpr float QSCALE = 0.08838834764831845f * 1.4426950408889634f;

constexpr unsigned WS_FLAG = 0;
constexpr unsigned WS_EPOCH = 16384;
constexpr unsigned WS_GRAN = 20480;
constexpr unsigned WS_TABB = 49152;
constexpr size_t WS_XH   = 65536;
constexpr size_t ACT_B   = (size_t)S_ * D_ * 2;
constexpr size_t WS_H2   = WS_XH + ACT_B;
constexpr size_t WS_X2   = WS_H2 + ACT_B;
constexpr size_t WS_P    = WS_X2 + 2 * ACT_B;
constexpr size_t WS_P2   = WS_P;
constexpr size_t WS_DBG  = WS_P + 32 * ACT_B;

DI unsigned rfl(unsigned x) { return __builtin_amdgcn_readfirstlane(x); }
DI unsigned cvtpk(float lo, float hi) { f32x2 v = {lo, hi}; bf16x2 b = __builtin_convertvector(v, bf16x2); return __builtin_bit_cast(unsigned, b); }
DI float bflo(unsigned w) { return __uint_as_float(w << 16); }
DI float bfhi(unsigned w) { return __uint_as_float(w & 0xffff0000u); }
DI float bfr(float x) { return bflo(cvtpk(x, x)); }
DI rsrc_t mkrsrc(const void* p, unsigned bytes) { return __builtin_amdgcn_make_buffer_rsrc((void*)p, (short)0, (int)bytes, 0x00020000); }
template <int OFF> DI void dma16(unsigned m0v, unsigned voff, rsrc_t r, unsigned soff) {
  asm volatile("s_mov_b32 m0, %0\n\ts_nop 0\n\tbuffer_load_dwordx4 %1, %2, %3 offen offset:%4 lds"
               :: "s"(m0v), "v"(voff), "s"(r), "s"(soff), "i"(OFF) : "memory");
}
DI void touch(unsigned m0v, unsigned voff, rsrc_t r) {
  asm volatile("s_mov_b32 m0, %0\n\ts_nop 0\n\tbuffer_load_dword %1, %2, 0 offen lds" :: "s"(m0v), "v"(voff), "s"(r) : "memory");
}
template <int OFF> DI void gld16(u32x4& d, unsigned voff, rsrc_t r, unsigned soff) {
  asm volatile("buffer_load_dwordx4 %0, %1, %2, %3 offen offset:%4" : "=v"(d) : "v"(voff), "s"(r), "s"(soff), "i"(OFF) : "memory");
}
template <int OFF> DI void gld16c(u32x4& d, unsigned voff, rsrc_t r, unsigned soff) {
  asm volatile("buffer_load_dwordx4 %0, %1, %2, %3 offen offset:%4 sc1" : "=v"(d) : "v"(voff), "s"(r), "s"(soff), "i"(OFF) : "memory");
}
template <bool FIRST> DI void mfma16(f32x4& c, bf16x8 a, u32x4 b) {
  if constexpr (FIRST) asm volatile("v_mfma_f32_16x16x32_bf16 %0, %1, %2, 0" : "=v"(c) : "v"(a), "v"(b));
  else asm volatile("v_mfma_f32_16x16x32_bf16 %0, %1, %2, %0" : "+v"(c) : "v"(a), "v"(b));
}
DI void prio_up(unsigned cond) { asm volatile("s_cmp_eq_u32 %0, 0\n\ts_cbranch_scc1 1f\n\ts_setprio 1\n1:" :: "s"(cond) : "scc"); }
DI s16x4 trd(lptr p) { return __builtin_amdgcn_ds_read_tr16_b64_v4i16((LAS s16x4*)p); }
DI bf16x8 trfrag(lptr p0, lptr p1) { s16x4 lo = trd(p0), hi = trd(p1); return __builtin_shufflevector(lo, hi, 0, 1, 2, 3, 4, 5, 6, 7); }
DI bf16x8 ldfrag(lptr p) { return *(LAS bf16x8*)p; }
DI bf16x8 asfrag(u32x4 w) { return __builtin_bit_cast(bf16x8, w); }
DI float swap_max(float m) { auto rr = __builtin_amdgcn_permlane32_swap(__float_as_uint(m), __float_as_uint(m), false, false);
  return __builtin_fmaxf(__uint_as_float(rr[0]), __uint_as_float(rr[1])); }
DI float swap_add(float m) { auto rr = __builtin_amdgcn_permlane32_swap(__float_as_uint(m), __float_as_uint(m), false, false);
  return __uint_as_float(rr[0]) + __uint_as_float(rr[1]); }
DI float swap16_add(float a, float b) { auto rr = __builtin_amdgcn_permlane16_swap(__float_as_uint(a), __float_as_uint(b), false, false);
  return __uint_as_float(rr[0]) + __uint_as_float(rr[1]); }
DI float swap32_add(float a, float b) { auto rr = __builtin_amdgcn_permlane32_swap(__float_as_uint(a), __float_as_uint(b), false, false);
  return __uint_as_float(rr[0]) + __uint_as_float(rr[1]); }
DI float ror8(float a) { return __int_as_float(__builtin_amdgcn_update_dpp(0, __float_as_int(a), 0x128, 0xf, 0xf, false)); }
DI void lbar() { asm volatile("s_waitcnt lgkmcnt(0)\n\ts_barrier" ::: "memory"); }
DI void signal(rsrc_t r, unsigned soff, u32x2 tag, unsigned prog, int tid) { if (tid == 0) __builtin_amdgcn_raw_buffer_store_b64(u32x2{tag[0] | prog, tag[1]}, r, 0, soff, 16); }
DI void gld8c(u32x2& v, unsigned voff, rsrc_t r, unsigned soff) { asm volatile("buffer_load_dwordx2 %0, %1, %2, %3 offen sc1" : "=v"(v) : "v"(voff), "s"(r), "s"(soff) : "memory"); }
DI bool all_lanes(bool c) { return __builtin_amdgcn_ballot_w64(c) == ~0ull; }
DI bool flag_ok(u32x2 v, u32x2 tag, unsigned need) { return v[1] == tag[1] && v[0] - (tag[0] + need) <= 15u - need; }
DI void poll_until(u32x2 v, unsigned voff, rsrc_t r, unsigned soff, u32x2 tag, unsigned need, unsigned own) {
  for (int sp = 0; !all_lanes(flag_ok(v, tag, need) || voff == own) && sp < (1 << 20); ++sp) {
    __builtin_amdgcn_s_sleep(2);
    asm volatile("buffer_load_dwordx2 %0, %1, %2, %3 offen sc1\n\ts_waitcnt vmcnt(0)" : "=v"(v) : "v"(voff), "s"(r), "s"(soff) : "memory");
  }
}
DI void wait_group(unsigned w, unsigned voff, rsrc_t r, unsigned soff, u32x2 tag, unsigned need, unsigned own) {
  if (w == 0) {
    const unsigned long long t = ((unsigned long long)tag[1] << 32) | tag[0], lo = t + need, hi = t + 15, om = __builtin_amdgcn_ballot_w64(voff == own);
    u32x2 a, b, c; unsigned n; unsigned long long m;
#define WG_LD(x) "buffer_load_dwordx2 " x ", %5, %6, %7 offen sc1\n\t"
#define WG_CK(x) "s_waitcnt vmcnt(2)\n\tv_cmp_le_u64 vcc, %8, " x "\n\tv_cmp_ge_u64 %4, %9, " x "\n\ts_and_b64 vcc, vcc, %4\n\ts_or_b64 vcc, vcc, %10\n\ts_cmp_eq_u64 vcc, -1\n\ts_cbranch_scc1 2f\n\t" WG_LD(x)
    asm volatile(WG_LD("%0") "s_sleep 6\n\t" WG_LD("%1") "s_sleep 6\n\t" WG_LD("%2") "s_mov_b32 %3, 0x80000\n1:\n\t"
                 WG_CK("%0") WG_CK("%1") WG_CK("%2") "s_sub_u32 %3, %3, 1\n\ts_cbranch_scc0 1b\n2:\n\ts_barrier\n\ts_waitcnt vmcnt(0)"
                 : "=&v"(a), "=&v"(b), "=&v"(c), "=&s"(n), "=&s"(m) : "v"(voff), "s"(r), "s"(soff), "s"(lo), "s"(hi), "s"(om) : "vcc", "scc", "memory");
#undef WG_LD
#undef WG_CK
  } else asm volatile("s_barrier" ::: "memory");
}
DI bf16x8 join16(unsigned a0, unsigned a1, unsigned b0, unsigned b1) {
  auto r0 = __builtin_amdgcn_permlane16_swap(a0, b0, false, false); auto r1 = __builtin_amdgcn_permlane16_swap(a1, b1, false, false);
  return __builtin_bit_cast(bf16x8, u32x4{r0[0], r1[0], r0[1], r1[1]});
}
DI u32x4 pack8(const f32x16& a, int s) {
  u32x4 w = {cvtpk(a[8 * s], a[8 * s + 1]), cvtpk(a[8 * s + 2], a[8 * s + 3]), cvtpk(a[8 * s + 4], a[8 * s + 5]), cvtpk(a[8 * s + 6], a[8 * s + 7])};
  return w;
}
DI lptr lp(unsigned a) { return (lptr)(uintptr_t)a; }
DI void bump(unsigned& a, int d) { a += d; PIN(a); }
template <int N> DI void wait_bar() { asm volatile("s_waitcnt vmcnt(%0) lgkmcnt(0)\n\ts_barrier" :: "i"(N) : "memory"); }
template <int I, int N, class F> DI void sfor(F&& f) { if constexpr (I < N) { f(std::integral_constant<int, I>{}); sfor<I + 1, N>(f); } }

#define STAMP(i)

constexpr int A_SLOT = 32768, A_SPARE = 4 * A_SLOT, A_LDS = 5 * A_SLOT, A_JUNK = A_SPARE + 16384;
constexpr float L_LIMIT = 1.2379400392853803e27f;
#define MARG 40.f
constexpr float M_MARGIN = MARG;

#define XNA 3
#define GU32 0
DI unsigned ncol(unsigned w, unsigned ln) { return 64 * (ln >> 5) + 16 * (w >> 1) + 4 * (w & 1) + 8 * ((ln >> 4) & 1) + 2 * ((ln >> 3) & 1); }
struct AttnState {
  f32x16 O[4], S[2], negm; u32x4 Pk[2][2]; bf16x8 Q[8], F[8]; float l;
};

constexpr int B_SLOT = 36864, B_SPARE = 4 * B_SLOT, SP_X = B_SPARE + 5120, SP_RED = B_SPARE + 14336;
constexpr int aslot(int C) { return (C < 19 ? C & 3 : (C & 3) == 3 ? 0 : (C & 3) == 0 ? 3 : C & 3) * A_SLOT; }
constexpr int bslot(int C) { return ((C + 2) & 3) * B_SLOT; }

__global__ __launch_bounds__(512, 2) void kmain(const u16* __restrict__ x, const u16* __restrict__ wq, const u16* __restrict__ kc,
                                                const u16* __restrict__ vc, const u16* __restrict__ wo, const u16* __restrict__ wg,
                                                const u16* __restrict__ wu, const u16* __restrict__ wd, const u16* __restrict__ cvec,
                                                const u16* __restrict__ wmod, char* __restrict__ ws, u16* __restrict__ out) {
  const u16* xh = (const u16*)(ws + WS_XH);
  extern __shared__ __attribute__((aligned(16))) char smem[];
  const lptr L = (lptr)smem;
  const unsigned lds0 = (unsigned)(uintptr_t)L;
  const int tid = threadIdx.x, lane = tid & 63;
  const unsigned w = rfl(tid >> 6), g = w & 3, hd = w >> 2;
  const unsigned j = 2 * (blockIdx.x & 7) + ((blockIdx.x >> 3) & 1), rb = blockIdx.x >> 4, rg = rb * 4 + g;
  const int r32 = lane & 31, h = lane >> 5, r8 = (lane >> 2) & 7, cp = lane & 3, blk = (lane >> 4) & 1, q4 = (lane >> 2) & 3;
  STAMP(0);
  const rsrc_t rWq = mkrsrc(wq, D_ * 4096 * 2), rK = mkrsrc(kc, HKV * SK * DH * 2), rV = mkrsrc(vc, HKV * SK * DH * 2),
               rWo = mkrsrc(wo, 4096 * D_ * 2), rX = mkrsrc(xh, S_ * D_ * 2);
  const unsigned voffWq = (16 * (w >> 1) + 8 * (r8 >> 2) + 4 * (w & 1) + (r8 & 3)) * 8192 + h * 64 + ((cp ^ ((r8 >> 2) * 2)) * 16) + 512 * j;
  const unsigned voffX = lane * 16 + rg * (NKS * 1024);
  const unsigned kvrow = 8 * (w >> 1) + r8;
  const unsigned voffV = kvrow * 256 + (2 * (w & 1) + h) * 64 + cp * 16;
  const unsigned voffK = kvrow * 256 + (2 * (w & 1) + h) * 64 + ((cp ^ ((kvrow >> 2) & 3)) * 16);
  const unsigned kvbase = (j * SK + (SK - WIN)) * 256;
  const unsigned voffWo = (256 * j + 32 * w + r8) * 1152 + h * 64 + cp * 16;
  const unsigned m0w4 = lds0 + w * 4096, m0w1 = lds0 + w * 1024;

  auto issue = [&](auto Cc, auto Pc) AI {
    constexpr int C = decltype(Cc)::value, P = decltype(Pc)::value, slot = aslot(C);
    if constexpr (C < 9) dma16<128 * P>(m0w4 + slot + P * 896, voffWq, rWq, C * 64 * 8192);
    else if constexpr (C < 17) {
      constexpr int i = C - 9;
      constexpr bool isK = (P < 2 && !(i == 7 && P == 1)) || (i == 0 && P == 2);
      constexpr int u = (i == 0 && P == 2) ? 0 : (i == 7 && P == 1) ? 15 : (P < 2 ? 2 * i + 1 + P : 2 * i - 3 + P);
      if constexpr (isK) dma16<0>(m0w1 + slot + P * 8192, voffK, rK, kvbase + u * 8192);
      else               dma16<0>(m0w1 + slot + P * 8192, voffV, rV, kvbase + u * 8192);
    } else if constexpr (C < 26) dma16<0>(m0w4 + slot + P * 1024, voffWo, rWo, (C - 17) * 128 + P * 8 * 1152);
  };
  u32x4 X[NKS];
  auto issueX = [&](auto Cc, auto Pc) AI {
    constexpr int C = decltype(Cc)::value, P = decltype(Pc)::value;
    if constexpr (C < 9) gld16c<1024 * P>(X[4 * C + P], voffX, rX, C * 4096);
  };
  const rsrc_t rC = mkrsrc(ws, 65536), rXr = mkrsrc(x, S_ * D_ * 2);
  const unsigned voffF = (lane & 15) * 8;
  unsigned long long pc0; unsigned clen;
  asm volatile("s_getpc_b64 %0\n.Lkm_begin:\n\ts_mov_b32 %1, .Lkm_end-.Lkm_begin" : "=s"(pc0), "=s"(clen));
  const rsrc_t rCode = mkrsrc((const void*)pc0, clen);
  if (w == 7) touch(lds0 + A_JUNK, ((blockIdx.x >> 3) * 32 + (lane & 31)) * 128, rCode);
  u32x2 tag;
  {
    const int e = lane >> 3, row8 = lane & 7;
    u32x2 ev; u32x4 wm[3], xa, xb; unsigned cv[3];
    asm volatile("buffer_load_dwordx2 %0, off, %1, %2 sc1" : "=v"(ev) : "s"(rC), "s"(WS_EPOCH) : "memory");
    dma16<0>(m0w1 + A_SPARE + 4096, e * 1152 + row8 * 16 + w * 128, rXr, (rb * 128 + j * 8) * 1152);
    gld16<1024>(xb, row8 * 1152 + w * 16, rXr, (rb * 128 + j * 8) * 1152);
    const rsrc_t rM = mkrsrc(wmod, D_ * 3456 * 2), rCv = mkrsrc(cvec, D_ * 2);
    const unsigned pb = ((blockIdx.x & 7) + 8 * (blockIdx.x >> 5)) * 4 + ((blockIdx.x >> 3) & 3);
    const unsigned vm = pb >= 216 ? 0x7fff0000u : (tid >> 1) * 6912 + (tid & 1) * 16 + pb * 32;
#pragma unroll
    for (int i = 0; i < 3; ++i) {
      gld16<0>(wm[i], vm, rM, i * 256 * 6912);
      asm volatile("buffer_load_ushort %0, %1, %2, %3 offen" : "=v"(cv[i]) : "v"(tid & ~1), "s"(rCv), "s"(i * 512) : "memory");
    }
    asm volatile("s_waitcnt vmcnt(0)" ::: "memory");
    if (w >= 6) {
      const unsigned u = tid - 384;
      touch(lds0 + A_JUNK, (36 * rb + (u >> 2)) * 8192 + 512 * j + (u & 3) * 128, rWq);
      if (w == 6) touch(lds0 + A_JUNK, (36 * rb + 32 + ((lane & 15) >> 2)) * 8192 + 512 * j + (u & 3) * 128, rWq);
    }
    asm volatile("" : "+v"(ev), "+v"(wm[0]), "+v"(wm[1]), "+v"(wm[2]), "+v"(cv[0]), "+v"(cv[1]), "+v"(cv[2]), "+v"(xb) :: "memory");
    xa = *(LAS u32x4*)(L + A_SPARE + 4096 + w * 1024 + row8 * 128 + e * 16);
    STAMP(16);
    { const unsigned long long t = (((unsigned long long)rfl(ev[1]) << 32) | rfl(ev[0])) + 16ull; tag[0] = (unsigned)t & ~15u; tag[1] = (unsigned)(t >> 32); }
    const unsigned gl = tag[0] << 12, gh = (tag[1] << 12) | (tag[0] >> 20);
    float a[8];
#pragma unroll
    for (int q = 0; q < 8; ++q) a[q] = 0.f;
#pragma unroll
    for (int i = 0; i < 3; ++i) {
      const float c0 = bflo(cv[i]), sc = bfr(c0 * __builtin_amdgcn_rcpf(1.f + __builtin_amdgcn_exp2f(c0 * -1.4426950408889634f)));
#pragma unroll
      for (int q = 0; q < 4; ++q) { a[2 * q] += sc * bflo(wm[i][q]); a[2 * q + 1] += sc * bfhi(wm[i][q]); }
    }
    const bool b3 = e & 1;
    const float a0 = swap32_add(a[0], a[4]), a1 = swap32_add(a[1], a[5]), a2 = swap32_add(a[2], a[6]), a3 = swap32_add(a[3], a[7]);
    const float b0 = swap16_add(a0, a2), b1 = swap16_add(a1, a3);
    float ms = (b3 ? b1 : b0) + ror8(b3 ? b0 : b1);
    ms += __shfl_xor(ms, 2); ms += __shfl_xor(ms, 4);
    const lptr red = L + A_SPARE;
    if ((lane & 6) == 0) *(LAS float*)(red + ((lane & 1) * 8 + e) * 32 + w * 4) = ms;
    float v[16];
#pragma unroll
    for (int q = 0; q < 4; ++q) { v[2 * q] = bflo(xa[q]); v[2 * q + 1] = bfhi(xa[q]); v[8 + 2 * q] = bflo(xb[q]); v[9 + 2 * q] = bfhi(xb[q]); }
    float s0 = 0.f, s1 = 0.f;
#pragma unroll
    for (int q = 0; q < 8; ++q) { s0 += v[q]; s1 += v[8 + q]; }
    s0 += e == 0 ? s1 : 0.f;
    s0 += ror8(s0); s0 = swap16_add(s0, s0); s0 = swap32_add(s0, s0);
    if (lane < 8) *(LAS float*)(red + 512 + lane * 32 + w * 4) = s0;
    lbar();
    auto sum8 = [&](lptr q) AI { const f32x4 r0 = *(LAS f32x4*)q, r1 = *(LAS f32x4*)(q + 16);
      return ((r0[0] + r0[1]) + (r0[2] + r0[3])) + ((r1[0] + r1[1]) + (r1[2] + r1[3])); };
    if (tid < 16 && pb < 216) {
      float m = bfr(sum8(red + tid * 32));
      const unsigned vec = pb / 36;
      if (vec == 1 || vec == 4) m = bfr(1.f + m);
      if (vec >= 2) {
        __builtin_amdgcn_raw_buffer_store_b16((u16)(__float_as_uint(m) >> 16), rC, tid * 2, WS_TABB + (pb - 72) * 32, 16);
        asm volatile("s_waitcnt vmcnt(0)" ::: "memory");
      }
      const u32x2 gr = {gl | (__float_as_uint(m) >> 16), gh};
      __builtin_amdgcn_raw_buffer_store_b64(gr, rC, tid * 8, WS_GRAN + pb * 128, 16);
    }
    STAMP(17);
    const float mean = sum8(red + 512 + row8 * 32) * (1.f / D_);
    s0 = 0.f; s1 = 0.f;
#pragma unroll
    for (int q = 0; q < 8; ++q) { v[q] -= mean; v[8 + q] -= mean; s0 += v[q] * v[q]; s1 += v[8 + q] * v[8 + q]; }
    s0 += e == 0 ? s1 : 0.f;
    s0 += ror8(s0); s0 = swap16_add(s0, s0); s0 = swap32_add(s0, s0);
    if (lane < 8) *(LAS float*)(red + 768 + lane * 32 + w * 4) = s0;
#define MIDC 2
    sfor<0, MIDC>([&](auto C) AI { sfor<0, 4>([&](auto P) AI { issue(C, P); }); });
    u32x2 g0, g1, g2;
    const lptr tab = red + 1024;
    if (w < 6) {
      for (int sp = 0; sp < (1 << 20); ++sp) {
        asm volatile("buffer_load_dwordx2 %0, %3, %4, %5 offen sc1\n\tbuffer_load_dwordx2 %1, %3, %4, %5 offen offset:3072 sc1\n\tbuffer_load_dwordx2 %2, %3, %4, %6 offen sc1\n\ts_waitcnt vmcnt(0)"
                     : "=&v"(g0), "=&v"(g1), "=&v"(g2) : "v"(tid * 8), "s"(rC), "s"(WS_GRAN), "s"(WS_GRAN + 6144) : "memory");
        if (all_lanes((((g0[0] ^ gl) | (g1[0] ^ gl) | (g2[0] ^ gl)) >> 16 | (g0[1] ^ gh) | (g1[1] ^ gh) | (g2[1] ^ gh)) == 0u)) break;
        __builtin_amdgcn_s_sleep(1);
      }
      *(LAS u16*)(tab + tid * 2) = (u16)g0[0]; *(LAS u16*)(tab + 768 + tid * 2) = (u16)g1[0]; *(LAS u16*)(tab + 1536 + tid * 2) = (u16)g2[0];
    }
    lbar();
    STAMP(15);
    const float rs = rsqrtf(sum8(red + 768 + row8 * 32) * (1.f / D_) + LN_EPS);
    const u32x4 sa = *(LAS u32x4*)(tab + w * 128 + e * 16), ca = *(LAS u32x4*)(tab + 1152 + w * 128 + e * 16);
    const u32x4 sb = *(LAS u32x4*)(tab + 1024 + w * 16), cb = *(LAS u32x4*)(tab + 1152 + 1024 + w * 16);
    u32x4 ha, hb;
#pragma unroll
    for (int q = 0; q < 4; ++q) {
      ha[q] = cvtpk((v[2 * q] * rs * bflo(ca[q]) + bflo(sa[q])) * QSCALE, (v[2 * q + 1] * rs * bfhi(ca[q]) + bfhi(sa[q])) * QSCALE);
      hb[q] = cvtpk((v[8 + 2 * q] * rs * bflo(cb[q]) + bflo(sb[q])) * QSCALE, (v[9 + 2 * q] * rs * bfhi(cb[q]) + bfhi(sb[q])) * QSCALE);
    }
    const unsigned ho = (rb * 4 + (j >> 2)) * 36864 + ((j >> 1) & 1) * 1024 + ((j & 1) * 8 + row8) * 16;
    __builtin_amdgcn_raw_buffer_store_b128(ha, rX, ho + (2 * w + (e >> 2)) * 2048 + (e & 3) * 256, 0, 16);
    if (lane < 8) __builtin_amdgcn_raw_buffer_store_b128(hb, rX, ho + (16 + (w >> 2)) * 2048 + (w & 3) * 256, 0, 16);
    asm volatile("s_waitcnt vmcnt(0)\n\ts_barrier" ::: "memory");
    STAMP(18);
    signal(rC, 7 * 2048 + rb * 128 + j * 8, tag, 1, tid);
    sfor<MIDC, 4>([&](auto C) AI { sfor<0, 4>([&](auto P) AI { issue(C, P); }); });
    if (w >= 1 && w < 4) {
      const unsigned vg = (tid < 208 ? tid - 64 : 0) * 128;
      for (int sp = 0; sp < (1 << 20); ++sp) {
        asm volatile("buffer_load_dwordx2 %0, %1, %2, %3 offen sc1\n\ts_waitcnt vmcnt(0)" : "=&v"(g1) : "v"(vg), "s"(rC), "s"(WS_GRAN + 1152 * 8) : "memory");
        if (all_lanes(((g1[0] ^ gl) >> 16 | (g1[1] ^ gh)) == 0u)) break;
        __builtin_amdgcn_s_sleep(1);
      }
    }
    wait_group(w, voffF, rC, 7 * 2048 + rb * 128, tag, 1, j * 8);
    STAMP(19);
  }
  sfor<0, 3>([&](auto C) AI { sfor<0, 4>([&](auto P) AI { issueX(C, P); }); });

  const int trb = h * 256 + q4 * 64 + blk * 32;
  unsigned aWq = lds0 + h * 8192 + (blk * 4 + q4) * 64 + blk * 32 + cp * 8 + hd * 2048, aWq1 = aWq ^ 32;
  PIN(aWq); PIN(aWq1);
  AttnState st;
  {
    f32x4 acc[8][2];
    auto stage = [&](auto Nc) AI {
      constexpr int n = decltype(Nc)::value, T = n & 7, C = n >> 4, ksl = (n >> 3) & 1;
      if constexpr ((n & 15) == 0) {
        if constexpr (C > 0) { bump(aWq, ((C & 3) - ((C - 1) & 3)) * A_SLOT); bump(aWq1, ((C & 3) - ((C - 1) & 3)) * A_SLOT); }
        constexpr int NV = C == 3 ? 12 : C == 8 ? 4 : 8;
        if constexpr (C >= 6) asm volatile("s_waitcnt lgkmcnt(0)" ::: "memory");
        asm volatile("s_waitcnt vmcnt(%4)\n\ts_barrier" : "+v"(X[4 * C]), "+v"(X[4 * C + 1]), "+v"(X[4 * C + 2]), "+v"(X[4 * C + 3]) : "i"(NV) : "memory");
      }
      const lptr b = lp((T & 1) ? aWq1 : aWq) + (4 * ksl * 8 + (T >> 1)) * 512;
      st.F[n & 7] = trfrag(b, b + 4096);
      if constexpr (n & 1) {
        using PC = std::integral_constant<int, ((n & 15) >> 2)>;
        if constexpr (n & 2) { if constexpr (C > 1 && C < 7) issue(std::integral_constant<int, C + 2>{}, PC{}); }
        else if constexpr (C < 6) issueX(std::integral_constant<int, C + 3>{}, PC{});
        else issue(std::integral_constant<int, C + 3>{}, PC{});
      }
    };
    sfor<0, 4>([&](auto N) AI { stage(N); SBAR(); });
    STAMP(1);
    sfor<0, 144>([&](auto Mc) AI {
      constexpr int m = decltype(Mc)::value;
      mfma16<(m < 8)>(acc[m & 7][0], st.F[m & 7], X[2 * (m >> 3)]);
      if constexpr (m + 4 < 144) stage(std::integral_constant<int, m + 4>{});
      mfma16<(m < 8)>(acc[m & 7][1], st.F[m & 7], X[2 * (m >> 3) + 1]);
      SBAR();
    });
    asm volatile("s_nop 7"); SBAR();
#pragma unroll
    for (int t = 0; t < 8; ++t)
      st.Q[t] = join16(cvtpk(acc[t][0][0], acc[t][0][1]), cvtpk(acc[t][0][2], acc[t][0][3]), cvtpk(acc[t][1][0], acc[t][1][1]), cvtpk(acc[t][1][2], acc[t][1][3]));
  }
  STAMP(2);
  unsigned aK0 = lds0 + (9 & 3) * A_SLOT + 2048 * (r32 >> 3) + 64 * (r32 & 7) + 16 * ((0 + h) ^ ((r32 >> 2) & 3));
  unsigned aK1 = aK0 ^ 32, aV = lds0 + (9 & 3) * A_SLOT + trb + cp * 8;
  PIN(aK0); PIN(aK1); PIN(aV);
#pragma unroll
  for (int t = 0; t < 4; ++t) st.O[t] = f32x16{};
  auto kfrag = [&](int off, auto Ic) AI { constexpr int i = decltype(Ic)::value; return ldfrag(lp((i & 1) ? aK1 : aK0) + off + 512 * (i >> 1)); };
  auto vfrag = [&](int off, auto Ic) AI { constexpr int i = decltype(Ic)::value, ks = i >> 2, T = i & 3;
    return trfrag(lp(aV) + off + ((2 * ks) * 4 + T) * 512, lp(aV) + off + ((2 * ks + 1) * 4 + T) * 512); };
  auto step = [&](auto Par, auto HQK, auto HPV, auto NXT, auto NVc, int u, int ko, int vo, int kn, int dsl, auto&& dmaf) AI {
    constexpr int par = decltype(Par)::value, nxt = decltype(NXT)::value, NV = decltype(NVc)::value;
    constexpr bool hqk = decltype(HQK)::value, hpv = decltype(HPV)::value;
    float l = st.l, eprev = 0.f;
    auto soft = [&](auto Rc) AI {
      constexpr int r = decltype(Rc)::value;
      const float e = __builtin_amdgcn_exp2f(st.S[par][r]);
      l += e; PIN(l);
      if constexpr (r & 1) { st.Pk[par][r >> 3][(r >> 1) & 3] = cvtpk(eprev, e); PIN(st.Pk[par][r >> 3]); }
      else { eprev = e; PIN(eprev); }
    };
    sfor<(hqk ? 0 : 8), (hpv ? 16 : 8)>([&](auto Mc) AI {
      constexpr int m = decltype(Mc)::value, n = (!hpv && m >= 4) ? m + 12 : m + 4;
      if constexpr (n < 8) st.F[n & 7] = kfrag(ko, std::integral_constant<int, n>{});
      else if constexpr (n < 16) st.F[n & 7] = vfrag(vo, std::integral_constant<int, n - 8>{});
      else {
        if constexpr (n == 16 && NV >= 0) { bump(aK0, dsl); bump(aK1, dsl); bump(aV, dsl); wait_bar<NV>(); }
        if constexpr (nxt == 1) st.F[n & 7] = kfrag(kn, std::integral_constant<int, n - 16>{});
        if constexpr (nxt == 2) st.F[n & 7] = vfrag(kn, std::integral_constant<int, n - 16>{});
        if constexpr (nxt == 3) st.F[n & 7] = vfrag(kn, std::integral_constant<int, ((n - 16) & 1) * 4 + ((n - 16) >> 1)>{});
      }
      dmaf(Mc);
      if constexpr (m < 8) st.S[par ^ 1] = MFMA(st.F[m & 7], st.Q[m], m == 0 ? st.negm : st.S[par ^ 1]);
      else st.O[m & 3] = MFMA(st.F[m & 7], asfrag(st.Pk[par ^ 1][(m - 8) >> 2]), st.O[m & 3]);
      if constexpr (hqk && hpv) soft(Mc);
      else { soft(std::integral_constant<int, 2 * (m & 7)>{}); soft(std::integral_constant<int, 2 * (m & 7) + 1>{}); }
      SBAR();
    });
    st.l = l;
  };
  using I0 = std::integral_constant<int, 0>; using I1 = std::integral_constant<int, 1>; using IM = std::integral_constant<int, -1>;
  using BT = std::true_type; using BF = std::false_type;
  {
    wait_bar<8>();
    f32x16 s = f32x16{};
    sfor<0, 8>([&](auto I) AI { s = MFMA(kfrag(2 * 8192, I), st.Q[decltype(I)::value], s); });
    sfor<0, 4>([&](auto I) AI { st.F[decltype(I)::value] = kfrag(0, I); });
    float mt = s[0];
#pragma unroll
    for (int r = 1; r < 16; ++r) mt = __builtin_fmaxf(mt, s[r]);
    mt = swap_max(mt) + M_MARGIN;
#pragma unroll
    for (int r = 0; r < 16; ++r) { st.S[0][r] = s[r] - mt; st.negm[r] = -mt; }
    PIN(st.negm);
    st.l = 0.f;
    SBAR();
  }
  auto nodma = [&](auto) AI {};
  auto warmF = [&](auto Uc) AI {
    constexpr int u = decltype(Uc)::value;
    if constexpr (u < 12) { if (w == (u & 3)) {
        unsigned t = lane; PIN(t); t += (u % 3) * 64;
        constexpr int junk = A_SPARE + (u & 3) * 8192;
        if constexpr (u < 3) touch(lds0 + junk, (256 * j + 16 * rb) * 1152 + (t < 144 ? t : 0) * 128, rWo);
        else if constexpr (u < 9) { const unsigned rr = t / 5, cc = t - rr * 5;
          touch(lds0 + junk, (36 * rb + (rr < 36 ? rr : 0)) * (F_ * 2) + ((576 * j) & ~127u) + cc * 128, mkrsrc(u < 6 ? wg : wu, D_ * F_ * 2)); }
        else touch(lds0 + junk, (288 * j + 18 * rb) * 1152 + (t < 162 ? t : 0) * 128, mkrsrc(wd, F_ * D_ * 2));
    } }
  };
  sfor<0, 8>([&](auto Ic) AI {
    constexpr int i = decltype(Ic)::value, C = 9 + i, dsl = (((C + 1) & 3) - (C & 3)) * A_SLOT;
    auto dmaf = [&](auto Mc) AI { constexpr int m = decltype(Mc)::value;
      if constexpr (i == 0) { if constexpr (m & 1) issue(std::integral_constant<int, 12>{}, std::integral_constant<int, (m >> 1)>{}); }
      else if constexpr (i < 7 && (m & 3) == 1) issue(std::integral_constant<int, C + 3>{}, std::integral_constant<int, (m >> 2)>{});
      if constexpr (m == (i > 0 ? 15 : 6)) warmF(std::integral_constant<int, 2 * i>{}); };
    if constexpr (i == 0) step(I0{}, BT{}, BF{}, I1{}, IM{}, 0, 0, 0, 8192, 0, dmaf);
    else if constexpr (i < 7) step(I0{}, BT{}, BT{}, I1{}, IM{}, 2 * i, 0, 2 * 8192, 8192, 0, dmaf);
    else step(I0{}, BT{}, BT{}, std::integral_constant<int, 2>{}, IM{}, 2 * i, 0, 2 * 8192, 3 * 8192, 0, dmaf);
    auto warm = [&](auto Mc) AI { constexpr int m = decltype(Mc)::value;
      if constexpr (m == 13) warmF(std::integral_constant<int, 2 * i + 1>{}); };
    if constexpr (i < 7) step(I1{}, BT{}, BT{}, I1{}, std::integral_constant<int, 8>{}, 2 * i + 1, 8192, 3 * 8192, 0, dsl, warm);
    else step(I1{}, BF{}, BT{}, std::integral_constant<int, 3>{}, IM{}, 15, 0, 3 * 8192, 8192, 0, nodma);
  });
  bf16x8 Of[16];
  {
    sfor<0, 8>([&](auto Ic) AI { constexpr int i = decltype(Ic)::value;
      if constexpr (i < 4) st.F[4 + i] = vfrag(8192, std::integral_constant<int, (i & 1) * 4 + 2 + (i >> 1)>{});
      st.O[i >> 1] = MFMA(st.F[i], asfrag(st.Pk[1][i & 1]), st.O[i >> 1]); SBAR(); });
    STAMP(3);
    if (__builtin_expect(__builtin_amdgcn_ballot_w64(!(st.l <= L_LIMIT)) != 0, 0)) {
      const u16* kg = kc + ((size_t)j * SK + (SK - WIN) + r32) * DH + 8 * h;
      const u16* vg = vc + ((size_t)j * SK + (SK - WIN) + 4 * h) * DH + r32;
      float mt = -__builtin_inff();
#pragma unroll 1
      for (int u = 0; u < 16; ++u) {
        f32x16 s = f32x16{};
#pragma unroll
        for (int i = 0; i < 8; ++i) s = MFMA(*(const bf16x8*)(kg + (size_t)u * 32 * DH + 16 * i), st.Q[i], s);
#pragma unroll
        for (int r = 0; r < 16; ++r) mt = __builtin_fmaxf(mt, s[r]);
      }
      mt = swap_max(mt);
      float l = 0.f;
#pragma unroll
      for (int t = 0; t < 4; ++t) st.O[t] = f32x16{};
#pragma unroll 1
      for (int u = 0; u < 16; ++u) {
        f32x16 s = f32x16{};
#pragma unroll
        for (int i = 0; i < 8; ++i) s = MFMA(*(const bf16x8*)(kg + (size_t)u * 32 * DH + 16 * i), st.Q[i], s);
#pragma unroll
        for (int r = 0; r < 16; ++r) { s[r] = __builtin_amdgcn_exp2f(s[r] - mt); l += s[r]; }
#pragma unroll
        for (int ks = 0; ks < 2; ++ks) {
          const bf16x8 pf = asfrag(pack8(s, ks));
#pragma unroll
          for (int t = 0; t < 4; ++t) {
            bf16x8 vf;
#pragma unroll
            for (int e = 0; e < 8; ++e) vf[e] = (short)vg[(size_t)(u * 32 + 16 * ks + 8 * (e >> 2) + (e & 3)) * DH + 32 * t];
            st.O[t] = MFMA(vf, pf, st.O[t]);
          }
        }
      }
      st.l = l;
    }
    const float inv = __builtin_amdgcn_rcpf(swap_add(st.l));
    asm volatile("s_waitcnt vmcnt(4)" ::: "memory");
    const lptr xw = L + (hd ? aslot(15) : A_SPARE) + g * 8192 + lane * 16, xr = L + (hd ? A_SPARE : aslot(15)) + g * 8192 + lane * 16;
#pragma unroll
    for (int t = 0; t < 4; ++t) {
#pragma unroll
      for (int r = 0; r < 16; ++r) st.O[t][r] *= inv;
      Of[2 * t] = asfrag(pack8(st.O[t], 0)); Of[2 * t + 1] = asfrag(pack8(st.O[t], 1));
      *(LAS bf16x8*)(xw + 2 * t * 1024) = Of[2 * t]; *(LAS bf16x8*)(xw + (2 * t + 1) * 1024) = Of[2 * t + 1];
    }
    wait_bar<4>();
#pragma unroll
    for (int i = 0; i < 8; ++i) Of[8 + i] = ldfrag(xr + i * 1024);
    SBAR();
  }
  STAMP(4);
  const unsigned hf = hd;
  const rsrc_t rGU = mkrsrc(hf ? wu : wg, D_ * F_ * 2), rWd = mkrsrc(wd, F_ * D_ * 2), rH2 = mkrsrc(ws + WS_H2, S_ * D_ * 2);
  unsigned lane2 = lane; PIN(lane2);
  unsigned voffGU[5];
  auto setGU = [&](auto NSc, auto Pc, unsigned ln) AI {
    constexpr int NS = decltype(NSc)::value, p = decltype(Pc)::value;
    const unsigned r8v = (ln >> 2) & 7;
    const unsigned piece = p < 4 ? g + 4 * p : 16 + (g & 1), stl = 2 * piece + (ln >> 5), R = stl / NS, sub = stl - NS * R;
    voffGU[p] = (8 * R + r8v) * (F_ * 2) + 576 * j + sub * 64 + (((ln & 3) ^ (GU32 ? hf * 2 : (r8v >> 2) * 2)) * 16);
    PIN(voffGU[p]);
  };
  sfor<0, 5>([&](auto P) AI { setGU(std::integral_constant<int, 9>{}, P, lane2); });
  const unsigned m0gu = lds0 + hf * 18432 + g * 1024, m0gux = lds0 + hf * 18432 + (16 + (g & 1)) * 1024;
  const bool xgu = (g >> 1) != 0;
  unsigned voffWd = 0;
  auto issueF = [&](auto Cc, auto Pc) AI {
    constexpr int C = decltype(Cc)::value, P = decltype(Pc)::value, slot = bslot(C);
    if constexpr (C < 18) {
      constexpr unsigned so = C < 12 ? C * 32 * F_ * 2 : (384 + 96 * (C & 1)) * F_ * 2 + 192 * ((C - 12) >> 1);
      if constexpr (P < 4) dma16<0>(m0gu + slot + P * 4096, voffGU[P], rGU, so);
      else { if (xgu == (bool)(C & 1)) dma16<0>(m0gux + slot, voffGU[4], rGU, so); }
    } else if constexpr (C < 27) {
      if constexpr (P < 4) dma16<0>(m0w1 + slot + P * 8192, voffWd, rWd, (C - 18) * 128 + P * 64 * 1152);
      else { if (xgu == (bool)(C & 1)) {
          const unsigned e = (g & 1) + 2 * hf;
          dma16<0>(lds0 + (32 + e) * 1024 + slot, voffWd, rWd, (C - 18) * 128 + (GU32 ? (32 + e - w) * 8 : 256 + 16 * hf + 4 * (g & 1) - 16 * (w >> 1) - 4 * (w & 1)) * 1152); } }
    }
  };
  const rsrc_t rP = mkrsrc(ws + WS_P, 16 * ACT_B);
  auto project = [&](auto NKc, auto CBc, const bf16x8* Bf, unsigned aOwn, unsigned aPar, auto&& dslot, auto&& nv, auto&& dma, auto&& preload, auto&& extra, auto&& early, float (&y)[10]) AI {
    constexpr int NK = decltype(NKc)::value, HK = NK / 2, CB = decltype(CBc)::value, NA = XNA;
    unsigned ln = lane; PIN(ln);
    const unsigned cons = 4 * g + ((ln & 31) >> 3);
    const unsigned voffP = rb * 16 * 147456 + (CB == 0 ? cons * 147456 + j * 128 : j * 147456 + cons * 128) + (4 * hd + (ln >> 5)) * 2048 + (ln & 7) * 16;
    const unsigned vo = rb * 16 * 147456 + w * 2048 + (CB == 0 ? j * 147456 + ln * 16 : (ln >> 3) * 147456 + j * 128 + (ln & 7) * 16);
    const unsigned co = rb * 128 + CB * 2048;
    PIN(aOwn); PIN(aPar);
    bf16x8 F[8]; f32x16 acc[2]; u32x4 pv[18]; u32x2 pl;
    const unsigned vf = (ln & 15) * 8;
    auto ldp = [&](auto Tc) AI { constexpr int t = decltype(Tc)::value; gld16c<0>(pv[t], vo, rP, (t >> 1) * 16384 + (t & 1) * (CB == 0 ? 1024 : 8 * 147456)); };
    f32x4 D[5]; u32x4 sel[2];
    auto mksel = [&]() AI {
#pragma unroll
      for (int d = 0; d < 4; ++d) {
        const unsigned v = (ln & 7) == 2 * d ? 0x3f80u : (ln & 7) == 2 * d + 1 ? 0x3f800000u : 0u;
        sel[0][d] = (ln & 8) ? 0u : v; sel[1][d] = (ln & 8) ? v : 0u;
      }
    };
    auto red = [&](auto Tc) AI {
      constexpr int t = decltype(Tc)::value;
      f32x4 c; if constexpr (t & 1) c = D[t >> 1]; else c = f32x4{};
      c = __builtin_amdgcn_mfma_f32_16x16x32_bf16(asfrag(sel[t & 1]), asfrag(pv[2 * t]), c, 0, 0, 0);
      D[t >> 1] = __builtin_amdgcn_mfma_f32_16x16x32_bf16(asfrag(sel[t & 1]), asfrag(pv[2 * t + 1]), c, 0, 0, 0);
      if constexpr ((t & 1) || t == 8) {
        const f32x4 d = D[t >> 1];
        const float v0 = d[0] + ror8(d[0]), v1 = d[1] + ror8(d[1]), v2 = d[2] + ror8(d[2]), v3 = d[3] + ror8(d[3]);
        y[t & ~1] = (ln & 8) ? v2 : v0; y[t | 1] = (ln & 8) ? v3 : v1;
      }
    };
    auto stage = [&](auto Nc) AI {
      constexpr int n = decltype(Nc)::value, i = n / NK, ks = n % NK;
      if constexpr (ks == 0 && i > 0) {
        bump(aOwn, dslot(i)); bump(aPar, dslot(i));
        constexpr int NV = decltype(nv(std::integral_constant<int, i>{}))::value;
        if constexpr (NA > 0 && i == NA + 5) {
          asm volatile("s_waitcnt vmcnt(%1)" : "+v"(pl) : "i"(NV) : "memory");
          poll_until(pl, vf, rC, co, tag, NA, j * 8);
          asm volatile("s_barrier" ::: "memory");
        } else asm volatile("s_waitcnt vmcnt(%0)\n\ts_barrier" :: "i"(NV) : "memory");
        if constexpr ((NA > 0 && i == NA + 2) || i == 8) signal(rC, co + j * 8, tag, i - 2, tid);
        if constexpr (NA > 0 && i == NA + 4) gld8c(pl, vf, rC, co);
      }
      const lptr b = lp(ks < HK ? aOwn : aPar) + (CB == 0 ? ks % HK : 6 * ((ks % HK) / 3) + ks % 3) * 2048;
      F[n & 7] = trfrag(b, b + 1024);
      if constexpr (NA > 0 && i == NA + 5 && ks < 2 * NA) ldp(std::integral_constant<int, ks>{});
      dma(std::integral_constant<int, i>{}, std::integral_constant<int, ks>{});
    };
    sfor<0, 4>([&](auto N) AI { stage(N); SBAR(); });
    sfor<0, 9 * NK>([&](auto Mc) AI {
      constexpr int m = decltype(Mc)::value, i = m / NK, ks = m % NK;
      if constexpr (m + 4 < 9 * NK) stage(std::integral_constant<int, m + 4>{});
      if constexpr (ks == 0) acc[i & 1] = MFMA(F[m & 7], Bf[0], f32x16{});
      else acc[i & 1] = MFMA(F[m & 7], Bf[ks], acc[i & 1]);
      if constexpr (i > 0 && (ks == 3 || ks == 5)) { constexpr int s = (ks - 3) >> 1;
        __builtin_amdgcn_raw_buffer_store_b128(pack8(acc[(i - 1) & 1], s), rP, voffP, (i - 1) * 16384 + s * 4096, 16); }
      SBAR();
    });
    __builtin_amdgcn_raw_buffer_store_b128(pack8(acc[0], 0), rP, voffP, 8 * 16384, 16);
    __builtin_amdgcn_raw_buffer_store_b128(pack8(acc[0], 1), rP, voffP, 8 * 16384 + 4096, 16);
    STAMP(CB == 0 ? 5 : 12);
    gld8c(pl, vf, rC, co);
    asm volatile("s_waitcnt vmcnt(0) lgkmcnt(0)\n\ts_barrier" : "+v"(pl) :: "memory");
    if constexpr (CB == 0) STAMP(30);
    signal(rC, co + j * 8, tag, 9, tid);
#pragma unroll
    for (int t = 0; t < 2 * NA; ++t) PIN(pv[t]);
    poll_until(pl, vf, rC, co, tag, 6, j * 8);
    sfor<2 * NA, 12>(ldp);
    extra();
    mksel();
    sfor<0, NA>(red);
    preload();
    wait_group(w, vf, rC, co, tag, 9, j * 8);
    STAMP(CB == 0 ? 6 : 13);
    sfor<12, 18>(ldp);
    asm volatile("s_waitcnt vmcnt(6)" ::: "memory");
#pragma unroll
    for (int t = 2 * NA; t < 12; ++t) PIN(pv[t]);
    sfor<NA, 6>(red);
    early();
    sfor<6, 9>([&](auto Tc) AI {
      constexpr int t = decltype(Tc)::value;
      asm volatile("s_waitcnt vmcnt(%2)" : "+v"(pv[2 * t]), "+v"(pv[2 * t + 1]) : "i"(16 - 2 * t) : "memory");
      red(Tc);
    });
  };
  auto rowsum = [&](float v, int slot) AI {
    v += ror8(v); v = swap16_add(v, v); v = swap32_add(v, v);
    if (lane < 8) *(LAS float*)(L + SP_RED + slot * 256 + lane * 32 + w * 4) = v;
    lbar();
    const f32x4 r0 = *(LAS f32x4*)(L + SP_RED + slot * 256 + (lane & 7) * 32), r1 = *(LAS f32x4*)(L + SP_RED + slot * 256 + (lane & 7) * 32 + 16);
    return ((r0[0] + r0[1]) + (r0[2] + r0[3])) + ((r1[0] + r1[1]) + (r1[2] + r1[3]));
  };
  {
    float x2[10], y[10], cf[10], sf[10], m0, s1, s2; unsigned xp[5], ga[5], sb[5], cb[5]; u32x4 tbv;
    const int e = lane >> 3, row8 = lane & 7;
    const lptr xk = L + SP_X + row8 * 1152 + ncol(w, lane) * 2;
    const unsigned aOwn = lds0 + (17 & 3) * A_SLOT + trb + cp * 8 + hd * 512 + hd * 16384;
    project(std::integral_constant<int, 16>{}, I0{}, Of, aOwn, aOwn ^ 16384,
            [](int i) AI { return aslot(17 + i) - aslot(16 + i); },
            [](auto Ic) AI { constexpr int i = decltype(Ic)::value;
              constexpr int ops[9] = {4, 6, 6, 6, 6, 6, 8, 2, 2}, NA = XNA;
              return std::integral_constant<int, ops[i - 1] + (NA > 0 && i - 1 == NA + 5 ? 2 * NA : 0)>{}; },
            [&](auto Ic, auto Kc) AI {
              constexpr int C = 17 + decltype(Ic)::value, ks = decltype(Kc)::value;
              if constexpr (C < 24 && (ks & 3) == 1) issue(std::integral_constant<int, C + 2>{}, std::integral_constant<int, (ks >> 2)>{});
              if constexpr (C == 23) {
                if constexpr (ks == 3) dma16<0>(m0w1 + SP_X, lane2 * 16 + w * 1024, rXr, (rb * 128 + j * 8) * 1152);
                if constexpr (ks == 11) gld16c<0>(tbv, (w < 5 ? w * 64 + lane2 : 0) * 16, rC, WS_TABB);
                if constexpr (ks == 15) { if (w == 0) dma16<0>(lds0 + SP_X + 8192, lane2 * 16, rXr, (rb * 128 + j * 8) * 1152 + 8192); }
              }
              if constexpr (C < 25) {}
              else if constexpr (ks == 8) {
                PIN(tbv);
                if (w < 5) *(LAS u32x4*)(L + B_SPARE + (w * 64 + lane2) * 16) = tbv;
              }
            },
            [&]() AI {
              const lptr tb = L + B_SPARE + ncol(w, lane2) * 2, xr = L + SP_X + (lane2 & 7) * 1152 + ncol(w, lane2) * 2;
#pragma unroll
              for (int i = 0; i < 5; ++i) {
                xp[i] = *(LAS unsigned*)(xr + i * 256); ga[i] = *(LAS unsigned*)(tb + i * 256);
                sb[i] = *(LAS unsigned*)(tb + 1152 + i * 256); cb[i] = *(LAS unsigned*)(tb + 2 * 1152 + i * 256);
                PIN(xp[i]); PIN(ga[i]); PIN(sb[i]); PIN(cb[i]);
              }
            },
            [&]() AI { sfor<0, 2>([&](auto C) AI { sfor<0, 5>([&](auto P) AI { issueF(C, P); }); }); },
            [&]() AI {
              float s = 0.f;
#pragma unroll
              for (int i = 0; i < 3; ++i) {
                x2[2 * i] = bflo(xp[i]) + bflo(ga[i]) * y[2 * i]; x2[2 * i + 1] = bfhi(xp[i]) + bfhi(ga[i]) * y[2 * i + 1];
                s += x2[2 * i] + x2[2 * i + 1];
                *(LAS unsigned*)(xk + i * 256) = cvtpk(x2[2 * i], x2[2 * i + 1]);
              }
              m0 = rowsum(s, 0) * (1.f / 384);
              s1 = 0.f; s2 = 0.f;
#pragma unroll
              for (int i = 0; i < 6; ++i) { x2[i] -= m0; s1 += x2[i]; s2 += x2[i] * x2[i]; PIN(x2[i]); }
#pragma unroll
              for (int i = 0; i < 5; ++i) {
                cf[2 * i] = bflo(cb[i]) * LOG2E; cf[2 * i + 1] = bfhi(cb[i]) * LOG2E; sf[2 * i] = bflo(sb[i]) * LOG2E; sf[2 * i + 1] = bfhi(sb[i]) * LOG2E;
                PIN(cf[2 * i]); PIN(cf[2 * i + 1]); PIN(sf[2 * i]); PIN(sf[2 * i + 1]);
              }
              PIN(s1); PIN(s2); PIN(m0);
            }, y);
    const bool tail = lane < 32;
#pragma unroll
    for (int i = 3; i < 5; ++i) {
      x2[2 * i] = bflo(xp[i]) + bflo(ga[i]) * y[2 * i]; x2[2 * i + 1] = bfhi(xp[i]) + bfhi(ga[i]) * y[2 * i + 1];
      xp[i] = cvtpk(x2[2 * i], x2[2 * i + 1]);
      x2[2 * i] -= m0; x2[2 * i + 1] -= m0;
      const float t1 = x2[2 * i] + x2[2 * i + 1], t2 = x2[2 * i] * x2[2 * i] + x2[2 * i + 1] * x2[2 * i + 1];
      s1 += (i < 4 || tail) ? t1 : 0.f; s2 += (i < 4 || tail) ? t2 : 0.f;
    }
    float sv = swap32_add(s1, s2); sv += ror8(sv); sv = swap16_add(sv, sv);
    if ((lane & 24) == 0) *(LAS float*)(L + SP_RED + 256 + row8 * 64 + (lane >> 5) * 32 + w * 4) = sv;
    lbar();
    const lptr rp = L + SP_RED + 256 + row8 * 64;
    const f32x4 r0 = *(LAS f32x4*)rp, r1 = *(LAS f32x4*)(rp + 16), r2 = *(LAS f32x4*)(rp + 32), r3 = *(LAS f32x4*)(rp + 48);
    *(LAS unsigned*)(xk + 3 * 256) = xp[3];
    if (tail) *(LAS unsigned*)(xk + 4 * 256) = xp[4];
    const float dm = (((r0[0] + r0[1]) + (r0[2] + r0[3])) + ((r1[0] + r1[1]) + (r1[2] + r1[3]))) * (1.f / D_);
    const float var = (((r2[0] + r2[1]) + (r2[2] + r2[3])) + ((r3[0] + r3[1]) + (r3[2] + r3[3]))) * (1.f / D_) - dm * dm;
    const float rstd = __builtin_amdgcn_rsqf(__builtin_fmaxf(var, 0.f) + LN_EPS);
    const lptr sc = L + bslot(3) + w * 1280;
#pragma unroll
    for (int i = 0; i < 5; ++i)
      *(LAS unsigned*)(sc + i * 256 + (lane >> 5) * 128 + row8 * 16 + (e & 3) * 4) =
          cvtpk((x2[2 * i] - dm) * rstd * cf[2 * i] + sf[2 * i], (x2[2 * i + 1] - dm) * rstd * cf[2 * i + 1] + sf[2 * i + 1]);
    asm volatile("s_waitcnt lgkmcnt(0)" ::: "memory");
    const u32x4 c0 = *(LAS u32x4*)(sc + lane * 16), c1 = *(LAS u32x4*)(sc + 1024 + row8 * 16);
    const unsigned ho = (rb * 4 + (j >> 2)) * 36864 + (GU32 ? w * 512 + ((j & 3) * 8 + row8) * 16 : (w >> 2) * 2048 + ((j >> 1) & 1) * 1024 + (w & 3) * 256 + ((j & 1) * 8 + row8) * 16);
    __builtin_amdgcn_raw_buffer_store_b128(c0, rH2, ho + e * 4096, 0, 16);
    if (lane < 8) __builtin_amdgcn_raw_buffer_store_b128(c1, rH2, ho + 8 * 4096, 0, 16);
    STAMP(7);
    if (blockIdx.x == 0 && w < 4) poll_until(u32x2{0u, ~tag[1]}, lane * 8, rC, 7 * 2048 + w * 512, tag, 1, ~0u);
    asm volatile("s_waitcnt vmcnt(0)\n\ts_barrier" ::: "memory");
    signal(rC, 3 * 2048 + rb * 128 + j * 8, tag, 1, tid);
    if (blockIdx.x == 0) signal(rC, WS_EPOCH, tag, 0, tid);
    wait_group(w, (lane & 15) * 8, rC, 3 * 2048 + rb * 128, tag, 1, j * 8);
    STAMP(8);
  }
  bf16x8 F[8], Af[18];
  unsigned lane3 = lane; PIN(lane3);
  const int trb3 = (lane3 >> 5) * 256 + ((lane3 >> 2) & 3) * 64 + (lane3 & 3) * 8, blk3 = (lane3 >> 4) & 1;
  {
    const unsigned voffH = lane3 * 16 + rg * (NKS * 1024);
    u32x4 H[NKS];
    auto issueH = [&](auto Ic) AI { constexpr int i = decltype(Ic)::value;
      gld16c<1024 * (i & 3)>(H[i], voffH, rH2, (i >> 2) * 4096);
    };
    sfor<0, 6>(issueH);
    const unsigned lb = lds0 + bslot(0) + (GU32 ? trb3 + blk3 * 18432 : (blk3 * 4 + ((lane3 >> 2) & 3)) * 64 + (lane3 & 3) * 8 + (lane3 >> 5) * 9216);
    unsigned aE = lb + hf * 512 + ((hf ^ blk3) * 32), aO = lb + hf * 1024 + ((1 ^ hf ^ blk3) * 32);
    PIN(aE); PIN(aO);
    { const unsigned r8v = (lane3 >> 2) & 7;
      voffWd = (288 * j + (GU32 ? 8 * w + r8v : 16 * (w >> 1) + 4 * (w & 1) + 8 * (r8v >> 2) + (r8v & 3))) * 1152 + (lane3 >> 5) * 64 + (lane3 & 3) * 16; }
    f32x4 acc[9][2][2];
    f32x16 acc32[9];
    struct GU { static constexpr int tile(int n) { return n < 216 ? (GU32 ? n % 9 : (n % 18) >> 1) : 3 * ((n / 18 - 12) >> 1) + (n % 18) / 6; }
                static constexpr int kstep(int n) { return n < 216 ? n / 18 : 12 + 3 * ((n / 18) & 1) + ((n % 6) >> 1); }
                static constexpr int k16(int n) { return n < 216 ? n / 9 : 24 + 6 * ((n / 18) & 1) + n % 6; }
                static constexpr int hops(int p) { return p < 9 ? 2 : p < 12 ? 4 : 0; } };
    auto stage = [&](auto Nc) AI {
      constexpr int n = decltype(Nc)::value, C = n / 18, r = n % 18, T = GU::tile(n), mm = n & 1;
      if constexpr (r == 0) {
        if constexpr (C > 0) {
          unsigned d = bslot(C) - bslot(C - 1);
          if constexpr (C == 12 && !GU32) d -= (lane3 >> 5) * 6144;
          aE += d; aO += d; PIN(aE); PIN(aO);
        }
        constexpr int NV = C == 0 ? 4 : C == 1 ? 8 : 4 + GU::hops(C - 1);
        if constexpr (C < 12) asm volatile("s_waitcnt vmcnt(%2)\n\ts_barrier" : "+v"(H[2 * C]), "+v"(H[2 * C + 1]) : "i"(NV) : "memory");
        else if constexpr (C < 14) { constexpr int b = 24 + 6 * (C - 12);
          asm volatile("s_waitcnt vmcnt(%6)\n\ts_barrier" : "+v"(H[b]), "+v"(H[b + 1]), "+v"(H[b + 2]), "+v"(H[b + 3]), "+v"(H[b + 4]), "+v"(H[b + 5]) : "i"(NV) : "memory"); }
        else asm volatile("s_waitcnt vmcnt(%0)\n\ts_barrier" :: "i"(NV) : "memory");
      }
#define PRIO_AT 12
      if constexpr (r == 0) prio_up(hf);
      if constexpr (r == PRIO_AT) __builtin_amdgcn_s_setprio(0);
      constexpr int off = GU32 ? (C < 12 ? 3 * (T / 3) + (T % 3) / 2 + 18 * (r / 9) : (T % 3) / 2 + 6 * (n % 6)) * 512
                               : mm * 18432 + (C < 12 ? 3 * (T / 3) + (T % 3) / 2 : (T % 3) / 2 + 12 * ((n % 6) >> 1)) * 512;
      const lptr b = lp(T % 3 == 1 ? aO : aE) + off;
      F[n & 7] = trfrag(b, b + (C < 12 ? 9 : 3) * 512);
      if constexpr (r % 4 == 0 && r / 4 < GU::hops(C)) issueH(std::integral_constant<int, (C < 9 ? 2 * C + 6 : 4 * C - 12) + r / 4>{});
      if constexpr (r % 4 == 2 || r == 16) issueF(std::integral_constant<int, C + 2>{}, std::integral_constant<int, r / 4>{});
      if constexpr (C == 9 && (r % 4 == 3 || r == 17)) { unsigned ln = lane; PIN(ln); setGU(std::integral_constant<int, 3>{}, std::integral_constant<int, r / 4>{}, ln); }
    };
    float e0, e1; unsigned pk[4];
    auto silu = [&](auto Gc, auto Sc) AI {
      constexpr int S = decltype(Sc)::value, u = S / 3, part = S % 3, T = 3 * decltype(Gc)::value + u / 4, rt = (u >> 1) & 1, d = u & 1, q = 2 * (u & 3);
      const float g0 = GU32 ? acc32[T][q] : acc[T][0][rt][2 * d], g1 = GU32 ? acc32[T][q + 1] : acc[T][0][rt][2 * d + 1];
      if constexpr (part == 0) {
        e0 = __builtin_amdgcn_exp2f(-g0); e1 = __builtin_amdgcn_exp2f(-g1);
        PIN(e0); PIN(e1);
      } else if constexpr (part == 1) {
        e0 = __builtin_amdgcn_rcpf(1.f + e0); e1 = __builtin_amdgcn_rcpf(1.f + e1);
        PIN(e0); PIN(e1);
      } else {
        const float u0 = GU32 ? acc32[T][8 + q] : acc[T][1][rt][2 * d], u1 = GU32 ? acc32[T][9 + q] : acc[T][1][rt][2 * d + 1];
        pk[u & 3] = cvtpk(g0 * u0 * e0, g1 * u1 * e1); PIN(pk[u & 3]);
        if constexpr ((u & 3) == 3) { Af[T] = GU32 ? asfrag(u32x4{pk[0], pk[1], pk[2], pk[3]}) : join16(pk[0], pk[1], pk[2], pk[3]); PIN(Af[T]); }
      }
    };
#define GPD 4
    sfor<0, GPD>([&](auto N) AI { stage(N); SBAR(); });
    STAMP(9);
    sfor<0, 324>([&](auto Mc) AI {
      constexpr int m = decltype(Mc)::value, T = GU::tile(m), ks = GU::kstep(m), mm = m & 1;
      if constexpr (GU32) { if constexpr (m < 9) acc32[T] = MFMA(F[m & 7], asfrag(H[GU::k16(m)]), f32x16{}); else acc32[T] = MFMA(F[m & 7], asfrag(H[GU::k16(m)]), acc32[T]); }
      else mfma16<(m < 18)>(acc[T][mm][0], F[m & 7], H[2 * ks]);
      if constexpr (m + GPD < 324) stage(std::integral_constant<int, m + GPD>{});
      if constexpr (m >= 241 && m < 313) silu(std::integral_constant<int, (m - 241) / 36>{}, std::integral_constant<int, (m - 241) % 36>{});
      if constexpr (m >= 313) silu(std::integral_constant<int, 2>{}, std::integral_constant<int, 2 * (m - 313)>{});
      if constexpr (!GU32) { SBAR(); mfma16<(m < 18)>(acc[T][mm][1], F[m & 7], H[2 * ks + 1]); }
      if constexpr (m >= 313) silu(std::integral_constant<int, 2>{}, std::integral_constant<int, 2 * (m - 313) + 1>{});
      SBAR();
    });
    STAMP(10);
    asm volatile("s_nop 7"); SBAR();
    lbar();
    const lptr xw = L + bslot(16) + w * 9216 + lane3 * 16, xr = L + bslot(16) + (w ^ 4) * 9216 + lane3 * 16;
#pragma unroll
    for (int t = 0; t < 7; ++t) *(LAS bf16x8*)(xw + t * 1024) = Af[t];
    sfor<22, 36>([&](auto Sc) AI { silu(std::integral_constant<int, 2>{}, Sc); });
    *(LAS bf16x8*)(xw + 7 * 1024) = Af[7]; *(LAS bf16x8*)(xw + 8 * 1024) = Af[8];
    wait_bar<4>();
#pragma unroll
    for (int t = 0; t < 9; ++t) Af[9 + t] = ldfrag(xr + t * 1024);
    SBAR();
  }
  STAMP(11);
  {
    float y[10]; unsigned gb[5], xp[5];
    const lptr im = L + (lane3 & 7) * 1152 + ncol(w, lane3) * 2;
    constexpr float GS = 1.f / (LOG2E * LOG2E);
    const unsigned aT = lds0 + bslot(18) + trb3 + blk3 * 32 + hf * 512;
    project(std::integral_constant<int, 18>{}, std::integral_constant<int, 4>{}, Af, aT + hf * 6144, aT + (1 - hf) * 6144,
            [](int i) AI { return bslot(18 + i) - bslot(17 + i); },
            [](auto Ic) AI { constexpr int i = decltype(Ic)::value;
              constexpr int ops[9] = {4, 6, 6, 6, 6, 6, 6, 2, 2}, NA = XNA;
              return std::integral_constant<int, ops[i - 1] + (NA > 0 && i - 1 == NA + 5 ? 2 * NA : 0)>{}; },
            [&](auto Ic, auto Kc) AI {
              constexpr int C = 18 + decltype(Ic)::value, ks = decltype(Kc)::value;
              if constexpr (C == 18) {
                if constexpr (ks == 8) asm volatile("s_barrier" ::: "memory");
                if constexpr (ks >= 9 && (ks & 1)) issueF(std::integral_constant<int, 20>{}, std::integral_constant<int, (ks - 9) / 2>{});
              } else if constexpr (ks >= 1 && ks < 16 && (ks % 3) == 1) issueF(std::integral_constant<int, C + 2>{}, std::integral_constant<int, (ks / 3)>{});
            },
            [&]() AI {
#pragma unroll
              for (int i = 0; i < 5; ++i) {
                gb[i] = *(LAS unsigned*)(L + B_SPARE + 3 * 1152 + ncol(w, lane3) * 2 + i * 256);
                xp[i] = *(LAS unsigned*)(L + SP_X + (lane3 & 7) * 1152 + ncol(w, lane3) * 2 + i * 256);
                PIN(gb[i]); PIN(xp[i]);
              }
            },
            [&]() AI {},
            [&]() AI {
#pragma unroll
              for (int i = 0; i < 3; ++i) *(LAS unsigned*)(im + i * 256) = cvtpk(bflo(xp[i]) + bflo(gb[i]) * GS * y[2 * i], bfhi(xp[i]) + bfhi(gb[i]) * GS * y[2 * i + 1]);
            }, y);
    unsigned ln = lane; PIN(ln);
#pragma unroll
    for (int i = 3; i < 5; ++i)
      if (i < 4 || ln < 32) *(LAS unsigned*)(im + i * 256) = cvtpk(bflo(xp[i]) + bflo(gb[i]) * GS * y[2 * i], bfhi(xp[i]) + bfhi(gb[i]) * GS * y[2 * i + 1]);
    lbar();
    u16* op = out + (size_t)(rb * 128 + j * 8) * D_;
    const unsigned t2 = w * 64 + ln;
#define OUT_AUX 16
    const rsrc_t rO = mkrsrc(op, 9216);
    __builtin_amdgcn_raw_buffer_store_b128(*(LAS u32x4*)(L + t2 * 16), rO, t2 * 16, 0, OUT_AUX);
    if (w == 0) __builtin_amdgcn_raw_buffer_store_b128(*(LAS u32x4*)(L + 8192 + ln * 16), rO, 8192 + ln * 16, 0, OUT_AUX);
  }
  asm volatile(".Lkm_end:");
  STAMP(14);
}

extern "C" void kernel_launch(void* const* d_in, const int* in_sizes, int n_in, void* d_out, int out_size, void* d_ws, size_t ws_size,
                              hipStream_t stream) {
  (void)in_sizes; (void)n_in; (void)out_size; (void)ws_size;
  const u16* x = (const u16*)d_in[0]; const u16* kc = (const u16*)d_in[1]; const u16* vc = (const u16*)d_in[2];
  const u16* wq = (const u16*)d_in[3]; const u16* wo = (const u16*)d_in[4];
  const u16* wg = (const u16*)d_in[6]; const u16* wu = (const u16*)d_in[7]; const u16* wd = (const u16*)d_in[8];
  const u16* c = (const u16*)d_in[9]; const u16* wmod = (const u16*)d_in[10];
  char* ws = (char*)d_ws;
  (void)hipFuncSetAttribute((const void*)kmain, hipFuncAttributeMaxDynamicSharedMemorySize, A_LDS);
  kmain<<<256, 512, A_LDS, stream>>>(x, wq, kc, vc, wo, wg, wu, wd, c, wmod, ws, (u16*)d_out);
}
```
